```python
import torch

def _softmax(x, axis=-1):
    return torch.softmax(x, dim=axis)
def _gelu(x):
    return torch.nn.functional.gelu(x, approximate="tanh")
def _silu(x):
    return torch.nn.functional.silu(x)
def _sigmoid(x):
    return torch.sigmoid(x)
def _layernorm(x, axis=-1, eps=1e-5):
    m = x.mean(dim=axis, keepdim=True)
    v = x.var(dim=axis, keepdim=True, unbiased=False)
    return (x - m) / torch.sqrt(v + eps)
def _scatter_add(tgt, idx, src, axis=0):
    return tgt.clone().index_add_(axis, idx, src)
def _segment_sum(src, seg, n):
    out = torch.zeros((n,) + src.shape[1:], dtype=src.dtype)
    return out.index_add_(0, seg, src)
def _pad(x, pw):
    flat = tuple(v for pair in reversed(pw) for v in pair)
    return torch.nn.functional.pad(x, flat)

kernel_name = 'gated_mix__linear_attn_block__dense_activation_squared_relu__prompt'

def setup_inputs():

    _w = (lambda a: (lambda t: t.to(torch.bfloat16) if t.is_floating_point() else t)(torch.as_tensor(a)))
    def _A_setup():
        B, S, D = 1, 2048, 512
        return {
            "x": _w((lambda *s: torch.randn(*s, dtype=torch.bfloat16) * 0.05)(B, S, D)),
            "wqkv": _w((lambda *s: torch.randn(*s, dtype=torch.bfloat16) * 0.8)(D, 3 * D)),
            "wo": _w((lambda *s: torch.randn(*s, dtype=torch.bfloat16) * 0.2)(D, D)),
        }
    _A_in = _A_setup()

    def _B_setup():
        B, S, D, F = 1, 2048, 512, 1792
        return {
            "x": _w((lambda *s: torch.randn(*s, dtype=torch.bfloat16) * 0.05)(B, S, D)),
            "w1": _w((lambda *s: torch.randn(*s, dtype=torch.bfloat16) * 0.1)(D, F)),
            "b1": _w((lambda *s: torch.randn(*s, dtype=torch.bfloat16) * 0.05)(F)),
            "w2": _w((lambda *s: torch.randn(*s, dtype=torch.bfloat16) * 0.05)(F, D)),
            "b2": _w((lambda *s: torch.randn(*s, dtype=torch.bfloat16) * 0.05)(D)),
        }
    _B_in = _B_setup()

    return {
        "A_x": _A_in["x"],
        "A_wqkv": _A_in["wqkv"],
        "A_wo": _A_in["wo"],
        "B_x": _B_in["x"],
        "B_w1": _B_in["w1"],
        "B_b1": _B_in["b1"],
        "B_w2": _B_in["w2"],
        "B_b2": _B_in["b2"],
        "gate_w": _w((lambda *s: torch.randn(*s, dtype=torch.bfloat16) * 0.05)(512) * 16.0),
    }

def reference(A_x, A_wqkv, A_wo, B_x, B_w1, B_b1, B_w2, B_b2, gate_w):
    def _A_ref(x, wqkv, wo):
        B, S, D = x.shape
        H = 32; Dh = D // H
        qkv = (lambda x, *d: x.permute(*d))((x @ wqkv).reshape(B, S, 3, H, Dh), 2, 0, 3, 1, 4)
        q, k, v = qkv[0], qkv[1], qkv[2]
        phi = lambda t: torch.where(t > 0, t + 1, torch.exp(t)) + 1e-6
        pq, pk = phi(q), phi(k)
        kv = torch.einsum("bhsd,bhse->bhde", pk, v)
        z = torch.einsum("bhsd->bhd", pk)
        num = torch.einsum("bhqd,bhde->bhqe", pq, kv)
        den = torch.einsum("bhqd,bhd->bhq", pq, z)[..., None] + 1e-6
        o = num / den
        return (lambda x, *d: x.permute(*d))(o, 0, 2, 1, 3).reshape(B, S, D) @ wo

    def _B_ref(x, w1, b1, w2, b2):
        h = torch.einsum("bsd,df->bsf", x, w1) + b1
        zero = torch.zeros_like(h)
        r = torch.where(h > zero, h, zero)
        h = r * r
        out = torch.einsum("bsf,fd->bsd", h, w2) + b2
        return out

    x = A_x
    g = _sigmoid((x * gate_w).sum(-1, keepdims=True))
    a = _A_ref(x, A_wqkv, A_wo)
    b = _B_ref(x, B_w1, B_b1, B_w2, B_b2)
    return g * a + (1 - g) * b
```

```cpp
#include <hip/hip_runtime.h>
#include <hip/hip_bf16.h>

#define LAS __attribute__((address_space(3)))
#define GAS __attribute__((address_space(1)))
#define DEVI __device__ __forceinline__
typedef __attribute__((ext_vector_type(8))) __bf16 bf16x8;
typedef __attribute__((ext_vector_type(2))) __bf16 bf16x2;
typedef __attribute__((ext_vector_type(4))) float f32x4;
typedef __attribute__((ext_vector_type(2))) float f32x2;
typedef __attribute__((ext_vector_type(4))) short s16x4;
typedef __attribute__((ext_vector_type(4))) unsigned u32x4;
typedef __attribute__((ext_vector_type(2))) unsigned u32x2;
typedef unsigned long long u64;
typedef __amdgpu_buffer_rsrc_t rsrc_t;

constexpr int SLOT = 16384;
constexpr int L_W = 0;
constexpr int L_A = 5 * SLOT;
constexpr int L_M = 9 * SLOT;
constexpr int L_GATE = L_M, L_B1 = L_M + 1024, L_B2 = L_M + 2048, L_Z = L_M + 3072;
constexpr int L_KVB = L_M + 4096;
constexpr int L_OUT = L_M + 8192;
constexpr int L_G64 = L_GATE;
constexpr int L_TOTAL = 10 * SLOT;
constexpr int WS_FLAG = 4096;
constexpr int F_H = 0;
constexpr int F_KV = 384;
constexpr int F_O = 640;
constexpr int WS_G = 128 * 1024;
constexpr int WS_Z = 144 * 1024;
constexpr int WS_KV = 256 * 1024;
constexpr int WS_ACT = 1024 * 1024;
constexpr int WS_END = WS_ACT + 128 * 72 * 1024;
constexpr int WS_DIAG = 16 * 1024 * 1024;

constexpr int PA = 13, PB = 14;
constexpr int CH1A = 12, CH1B = 13;
constexpr int PH1 = 12;
constexpr int PH2A = 15, PH2B = 17;
constexpr int KV_LDS = 10;
constexpr int KV_PUB = 13;
constexpr int KV_POLL = 15;
constexpr int KV_RDY = 18;
constexpr int PO = 21;
constexpr int LAG = 5, NBUF = 6;

template <int I> struct IC { static constexpr int v = I; };
template <int B, int E, class F> DEVI void sfor(F&& f) {
  if constexpr (B < E) { f(IC<B>{}); sfor<B + 1, E>(f); }
}
#define STAMP(i) do {} while (0)
#define STAMPR(i) do {} while (0)
DEVI void bar() {
  __builtin_amdgcn_sched_barrier(0);
  asm volatile("s_waitcnt lgkmcnt(0)\n\ts_barrier" ::: "memory");
  __builtin_amdgcn_sched_barrier(0);
}
template <int N> DEVI void vmwait() { asm volatile("s_waitcnt vmcnt(%0)" :: "i"(N) : "memory"); }
DEVI void sb() { __builtin_amdgcn_sched_barrier(0); }
DEVI int lane_id() { int l; asm volatile("v_mbcnt_lo_u32_b32 %0, -1, 0\n\tv_mbcnt_hi_u32_b32 %0, -1, %0" : "=v"(l)); return l; }
DEVI rsrc_t mkrs(const void* p, unsigned n) { return __builtin_amdgcn_make_buffer_rsrc((void*)p, 0, n, 0x00020000); }
DEVI void dma(unsigned lds, unsigned voff, rsrc_t rs, unsigned soff) {
  asm volatile("s_mov_b32 m0, %0\n\ts_nop 0\n\tbuffer_load_dwordx4 %1, %2, %3 offen lds" :: "s"(lds), "v"(voff), "s"(rs), "s"(soff) : "memory");
}
DEVI void dma_sc1(unsigned lds, unsigned voff, rsrc_t rs, unsigned soff) {
  asm volatile("s_mov_b32 m0, %0\n\ts_nop 0\n\tbuffer_load_dwordx4 %1, %2, %3 offen sc1 lds" :: "s"(lds), "v"(voff), "s"(rs), "s"(soff) : "memory");
}
DEVI bf16x8 rd_frag(unsigned a) { return *(const LAS bf16x8*)(size_t)a; }
DEVI bf16x8 rd_tr(unsigned a, unsigned d) {
  s16x4 lo = __builtin_amdgcn_ds_read_tr16_b64_v4i16((LAS s16x4*)(size_t)a);
  s16x4 hi = __builtin_amdgcn_ds_read_tr16_b64_v4i16((LAS s16x4*)(size_t)(a + d));
  return __builtin_bit_cast(bf16x8, __builtin_shufflevector(lo, hi, 0, 1, 2, 3, 4, 5, 6, 7));
}
DEVI f32x4 mfma(bf16x8 a, bf16x8 b, f32x4 c) { return __builtin_amdgcn_mfma_f32_16x16x32_bf16(a, b, c, 0, 0, 0); }
DEVI unsigned pk(float a, float b) { f32x2 v = {a, b}; return __builtin_bit_cast(unsigned, __builtin_convertvector(v, bf16x2)); }
DEVI u32x4 pk8(f32x4 a, f32x4 b) { return u32x4{pk(a[0], a[1]), pk(a[2], a[3]), pk(b[0], b[1]), pk(b[2], b[3])}; }
DEVI f32x4 unpk4(u32x2 v) {
  return f32x4{__uint_as_float(v[0] << 16), __uint_as_float(v[0] & 0xffff0000u), __uint_as_float(v[1] << 16), __uint_as_float(v[1] & 0xffff0000u)};
}
DEVI f32x4 relu2(f32x4 a) { f32x4 r; for (int i = 0; i < 4; ++i) { r[i] = fmaxf(a[i], 0.f); } return r * r; }
DEVI f32x4 phi(f32x4 a) { f32x4 r; for (int i = 0; i < 4; ++i) r[i] = (a[i] > 0.f ? a[i] + 1.f : __expf(a[i])) + 1e-6f; return r; }
DEVI void st_sc1(u32x4 v, rsrc_t rs, unsigned voff, unsigned soff) { __builtin_amdgcn_raw_buffer_store_b128(v, rs, voff, soff, 16); }
DEVI u32x4 ld_sc1(rsrc_t rs, unsigned voff, unsigned soff) { return __builtin_amdgcn_raw_buffer_load_b128(rs, voff, soff, 16); }
DEVI void set_flag(char* ws, int idx, u64 tag) {
  if ((threadIdx.x & 63) == 0) __hip_atomic_store((GAS u64*)(ws + WS_FLAG + idx * 128), tag, __ATOMIC_RELAXED, __HIP_MEMORY_SCOPE_AGENT);
}
DEVI void poll(char* ws, int idx, u64 tag) {
  const GAS u64* p = (const GAS u64*)(ws + WS_FLAG + idx * 128);
  for (unsigned spins = 0; spins < (1u << 17); ++spins) {
    u64 v = __hip_atomic_load(p, __ATOMIC_RELAXED, __HIP_MEMORY_SCOPE_AGENT);
    if (__all(v == tag)) break;
    __builtin_amdgcn_s_sleep(1);
  }
  asm volatile("" ::: "memory");
}

template <bool KA>
DEVI void compute(char* p_ws, __hip_bfloat16* p_out, int w, int rt, int j, int mt, int nt) {
  const rsrc_t rws = mkrs(p_ws, WS_END);
  constexpr int P = KA ? PA : PB;
  u32x4 pq[2][2];
  float gq[2];
  {
    constexpr int NQ = KA ? 192 : 224;
    bf16x8 xf[16][2], wf[NBUF], gf[2];
    f32x4 acc[8][2], acc2[4][2], acc3[2][2], accg[2];
    u32x4 pkf[4], pvf[4];
    u32x2 bt;
    f32x4 bu = {0, 0, 0, 0};
    unsigned wc[4], xb[2], bb, actv;
    {
      const int lane = lane_id(), g = lane >> 4, q = (lane & 15) >> 2, pp = lane & 3, f = (q >> 1) | ((g & 1) << 1);
      for (int c = 0; c < 4; ++c) wc[c] = L_W + (8 * g + q) * 128 + ((c ^ f) * 32) + pp * 8;
      for (int k = 0; k < 2; ++k) xb[k] = L_A + (32 * w + (lane & 15)) * 128 + (((4 * k + g) ^ ((lane & 15) >> 1)) * 16);
      bb = KA ? L_GATE + 16 * g : L_B1 + 8 * g + (j & 1) * 64;
    }
    const unsigned acts = WS_ACT + (rt * 8 + 2 * w) * (72 * 1024);
    sfor<0, NQ + LAG>([&](auto ic) {
      constexpr int n = ic.v;
      if constexpr (n < NQ) {
        constexpr int st = n >> 4, r = n & 15;
        if constexpr (r == 0) {
          if constexpr (!KA && st == PH1) vmwait<0>();
          bar();
          STAMP(st);
          if constexpr (st == 12) {
            const int lane = lane_id(), g = lane >> 4, q = (lane & 15) >> 2, pp = lane & 3;
            for (int c = 0; c < 2; ++c) wc[c] = L_W + (st % 5) * SLOT + (8 * g + q) * 64 + ((c ^ (g & 1)) * 32) + pp * 8;
            bb = L_B1 + 8 * g + ((j & 1) ? 0 : 384);
          } else if constexpr (st > 0) {
            for (int c = 0; c < (st < 12 ? 4 : 2); ++c) { wc[c] += (st % 5) ? SLOT : -4 * SLOT; asm volatile("" : "+v"(wc[c])); }
          }
          if constexpr (st == 0) {
            for (int t = 0; t < 8; ++t) {
              f32x4 b = KA ? f32x4{0, 0, 0, 0} : unpk4(*(const LAS u32x2*)(size_t)(bb + t * 32));
              acc[t][0] = b; acc[t][1] = b;
            }
            accg[0] = accg[1] = f32x4{0, 0, 0, 0};
          }
          if constexpr (st == 12)
            for (int t = 0; t < 2; ++t) { f32x4 b = unpk4(*(const LAS u32x2*)(size_t)(bb + t * 32)); acc3[t][0] = b; acc3[t][1] = b; }
        }
        if constexpr (st < 8) {
          constexpr int kk = r >> 3, t = r & 7;
          if constexpr (t == 0) {
            for (int mi = 0; mi < 2; ++mi) xf[2 * st + kk][mi] = rd_frag(xb[kk] + (st & 3) * SLOT + mi * 2048);
            if constexpr (KA) gf[kk] = rd_frag(bb + (2 * st + kk) * 64);
          }
          wf[n % NBUF] = rd_tr(wc[t & 3] + (t >> 2) * 8192 + kk * 4096, 512);
        } else if constexpr (st == 8) {
          if constexpr (!KA && (r & 3) == 0) bt = *(const LAS u32x2*)(size_t)(bb + 256 + (r >> 2) * 32);
          if constexpr (!KA && (r & 3) == 3) bu = unpk4(bt);
          wf[n % NBUF] = rd_tr(wc[r >> 2] + (r & 3) * 4096, 512);
        } else if constexpr (st < 12) {
          wf[n % NBUF] = rd_tr(wc[r & 3] + (r >> 2) * 4096, 512);
        } else {
          wf[n % NBUF] = rd_tr(wc[r & 1] + (r >> 1) * 2048, 256);
        }
      }
      constexpr int m = n - LAG;
      if constexpr (m >= 0) {
        constexpr int st = m >> 4, r = m & 15;
        if constexpr (st < 8) {
          constexpr int kk = r >> 3, t = r & 7, ks = 2 * st + kk;
          for (int mi = 0; mi < 2; ++mi)
            acc[t][mi] = KA ? mfma(xf[ks][mi], wf[m % NBUF], acc[t][mi]) : mfma(wf[m % NBUF], xf[ks][mi], acc[t][mi]);
          if constexpr (KA && t == 1) for (int mi = 0; mi < 2; ++mi) accg[mi] = mfma(gf[kk], xf[ks][mi], accg[mi]);
        } else if constexpr (st == 8) {
          static_assert(LAG >= 3 && LAG <= 6, "bias register hand-over");
          constexpr int ks = r & 3, t = r >> 2;
          for (int mi = 0; mi < 2; ++mi) acc2[t][mi] = mfma(wf[m % NBUF], xf[ks][mi], ks ? acc2[t][mi] : bu);
        } else if constexpr (st < 12) {
          constexpr int ks = 4 * (st - 8) + (r >> 2), t = r & 3;
          for (int mi = 0; mi < 2; ++mi) acc2[t][mi] = mfma(wf[m % NBUF], xf[ks][mi], acc2[t][mi]);
        } else {
          constexpr int ks = 8 * (st - 12) + (r >> 1), t = r & 1;
          for (int mi = 0; mi < 2; ++mi) acc3[t][mi] = mfma(wf[m % NBUF], xf[ks][mi], acc3[t][mi]);
        }
        if constexpr (m == 123) actv = lane_id() * 16;
        if constexpr (!KA) {
          if constexpr (m >= 124 && m < 124 + 16 && (m & 1) == 0) {
            constexpr int e = (m - 124) >> 1, pr = e >> 1, mi = e & 1;
            st_sc1(pk8(relu2(acc[2 * pr][mi]), relu2(acc[2 * pr + 1][mi])), rws, actv, acts + (mi * 72 + 4 * j + pr) * 1024);
          }
          if constexpr (m >= 192 && m < 192 + 16 && (m & 3) == 0) {
            constexpr int e = (m - 192) >> 2, pr = e >> 1, mi = e & 1;
            st_sc1(pk8(relu2(acc2[2 * pr][mi]), relu2(acc2[2 * pr + 1][mi])), rws, actv, acts + (mi * 72 + 32 + 2 * j + pr) * 1024);
          }
        } else {
          if constexpr (m >= 126 && m < 130) pvf[m - 126] = pk8(acc[m - 122][0], acc[m - 122][1]);
          if constexpr (m >= 124 && m < 124 + 24 && (m - 124) % 3 == 0) {
            constexpr int h = (m - 124) / 6, mi = ((m - 124) / 3) & 1;
            const f32x4 v = phi(acc[h][mi]);
            pkf[h][2 * mi] = pk(v[0], v[1]); pkf[h][2 * mi + 1] = pk(v[2], v[3]);
          }
          if constexpr (m >= 130 && m < 130 + 24 && (m - 130) % 6 == 0) {
            constexpr int h = (m - 130) / 6;
            const bf16x8 kf = __builtin_bit_cast(bf16x8, pkf[h]);
            const bf16x8 vf = __builtin_bit_cast(bf16x8, pvf[h]);
            const bf16x8 one = __builtin_bit_cast(bf16x8, u32x4{0x3f803f80u, 0x3f803f80u, 0x3f803f80u, 0x3f803f80u});
            f32x4 kv = mfma(kf, vf, f32x4{0, 0, 0, 0}), z = mfma(kf, one, f32x4{0, 0, 0, 0});
            *(LAS f32x4*)(size_t)(L_A + (h * 4 + w) * 1024 + actv) = kv;
            *(LAS f32x4*)(size_t)(bb + (L_Z - L_GATE) + (h * 4 + w) * 64) = z;
          }
        }
      }
      sb();
    });
    if constexpr (KA) {
      bar();
      STAMP(12);
      for (int mi = 0; mi < 2; ++mi) {
        gq[mi] = __builtin_amdgcn_rcpf(1.f + __expf(-accg[mi][0]));
        for (int hp = 0; hp < 2; ++hp) pq[mi][hp] = pk8(phi(acc2[2 * hp][mi]), phi(acc2[2 * hp + 1][mi]));
      }
    } else {
      for (int mi = 0; mi < 2; ++mi)
        st_sc1(pk8(relu2(acc3[0][mi]), relu2(acc3[1][mi])), rws, actv, acts + (mi * 72 + 48 + j) * 1024);
    }
  }
  const int lane = lane_id(), g = lane >> 4, q = (lane & 15) >> 2, pp = lane & 3, f = (q >> 1) | ((g & 1) << 1);
  const int wm = w & 1, wn = w >> 1;
  f32x4 o[2][2];
  for (int t = 0; t < 2; ++t) { f32x4 b = unpk4(*(const LAS u32x2*)(size_t)(L_B2 + nt * 128 + wn * 64 + t * 32 + g * 8)); o[t][0] = b; o[t][1] = b; }
  unsigned w2b[2];
  for (int c = 0; c < 2; ++c) w2b[c] = L_W + (4 * g + q) * 128 + (((2 * wn + c) ^ f) * 32) + pp * 8;
  const unsigned a2b = L_A + wm * 2048 + lane * 16;
  auto stage2 = [&](int t) {
    const unsigned ws_ = ((t + (KA ? 12 : 14)) % 5) * SLOT, as_ = (t & 3) * SLOT;
    const unsigned wa0 = w2b[0] + ws_, wa1 = w2b[1] + ws_, aa = a2b + as_;
    bf16x8 af[4][2], bf[4][2];
    for (int u = 0; u < 4; ++u) {
      af[u][0] = rd_frag(aa + u * 4096); bf[u][0] = rd_tr(wa0 + u * 4096, 2048);
      af[u][1] = rd_frag(aa + u * 4096 + 1024); bf[u][1] = rd_tr(wa1 + u * 4096, 2048);
    }
    for (int u = 0; u < 4; ++u)
      for (int c = 0; c < 2; ++c)
        for (int mi = 0; mi < 2; ++mi) o[c][mi] = mfma(bf[u][c], af[u][mi], o[c][mi]);
  };
#pragma unroll 1
  for (int t = 0; t < 18; ++t) {
    const int i = t + P;
    if (KA ? (i == PO) : (i == PH2A || i == PH2B)) {
      if (!KA && i == PH2A) vmwait<2>(); else vmwait<0>();
    }
    bar();
    STAMP(i);
    if (t == 14) {
      for (int mi = 0; mi < 2; ++mi) {
        const float s = 1.f - *(const LAS float*)(size_t)(L_G64 + (wm * 32 + mi * 16 + (lane & 15)) * 4);
        o[0][mi] *= s; o[1][mi] *= s;
      }
    }
    stage2(t);
    if (KA && i == KV_RDY) {
      const unsigned acts = WS_ACT + (rt * 8 + 2 * w) * (72 * 1024);
      for (int hp = 0; hp < 2; ++hp) {
        const u32x4 k0 = *(const LAS u32x4*)(size_t)(L_KVB + (2 * hp) * 1024 + lane * 16);
        const u32x4 k1 = *(const LAS u32x4*)(size_t)(L_KVB + (2 * hp + 1) * 1024 + lane * 16);
        const bf16x8 kv0 = __builtin_bit_cast(bf16x8, u32x4{k0[0], k0[1], 0, 0}), z0 = __builtin_bit_cast(bf16x8, u32x4{k0[2], k0[3], 0, 0});
        const bf16x8 kv1 = __builtin_bit_cast(bf16x8, u32x4{0, 0, k1[0], k1[1]}), z1 = __builtin_bit_cast(bf16x8, u32x4{0, 0, k1[2], k1[3]});
        for (int mi = 0; mi < 2; ++mi) {
          const bf16x8 pf = __builtin_bit_cast(bf16x8, pq[mi][hp]);
          const f32x4 zero = {0, 0, 0, 0};
          f32x4 n0 = mfma(kv0, pf, zero), d0 = mfma(z0, pf, zero), n1 = mfma(kv1, pf, zero), d1 = mfma(z1, pf, zero);
          n0 *= gq[mi] * __builtin_amdgcn_rcpf(d0[0] + 1e-6f);
          n1 *= gq[mi] * __builtin_amdgcn_rcpf(d1[0] + 1e-6f);
          st_sc1(pk8(n0, n1), rws, lane * 16, acts + (mi * 72 + 56 + 2 * j + hp) * 1024);
        }
      }
      if (j == 0 && lane < 32)
        __builtin_amdgcn_raw_buffer_store_b32(__float_as_uint(lane < 16 ? gq[0] : gq[1]), rws, lane * 4, WS_G + (rt * 128 + 32 * w) * 4, 16);
    }
  }
  STAMP(40);
  const unsigned ob = L_OUT + w * 2048;
  for (int mi = 0; mi < 2; ++mi)
    for (int c = 0; c < 2; ++c) {
      const int row = mi * 16 + (lane & 15);
      *(LAS u32x2*)(size_t)(ob + row * 64 + (((2 * c + (g >> 1)) ^ ((row >> 1) & 3)) * 16) + (g & 1) * 8) =
          u32x2{pk(o[c][mi][0], o[c][mi][1]), pk(o[c][mi][2], o[c][mi][3])};
    }
  for (int h = 0; h < 2; ++h) {
    const int row = h * 16 + (lane >> 2);
    const u32x4 v = *(const LAS u32x4*)(size_t)(ob + row * 64 + (((lane & 3) ^ ((row >> 1) & 3)) * 16));
    *(u32x4*)((char*)p_out + (size_t)(mt * 64 + wm * 32 + row) * 1024 + nt * 128 + wn * 64 + (lane & 3) * 16) = v;
  }
  STAMP(41);
  STAMPR(61);
}

__global__ void __launch_bounds__(512, 2)
fused_kernel(const char* p_x, const char* p_wqkv, const char* p_wo, const char* p_w1, const char* p_b1,
             const char* p_w2, const char* p_b2, const char* p_gate, __hip_bfloat16* p_out, char* p_ws) {
  __shared__ __attribute__((aligned(16))) char smem[L_TOTAL];
  asm volatile("" :: "v"((LAS char*)smem) : "memory");
  const int lane = threadIdx.x & 63, w = __builtin_amdgcn_readfirstlane(threadIdx.x >> 6);
  const int b = blockIdx.x, xcd = b & 7, bi = b >> 3;
  const int rt = (xcd & 1) * 8 + (bi & 7), j = (xcd >> 1) * 2 + (bi >> 4);
  const bool ka = ((bi >> 3) & 1) == 0;
  const int mt = xcd * 4 + (bi & 3), nt = bi >> 2;
  STAMPR(60);
  STAMP(62);
  if (w < 4) {
    if (ka) compute<true>(p_ws, p_out, w, rt, j, mt, nt);
    else compute<false>(p_ws, p_out, w, rt, j, mt, nt);
  } else if (w == 4) {
    const unsigned pitch = ka ? 3072 : 3584;
    const rsrc_t r1 = ka ? mkrs(p_wqkv, 512 * 3072) : mkrs(p_w1, 512 * 3584);
    const rsrc_t r2 = mkrs(p_w2, 1792 * 1024), ro = mkrs(p_wo, 512 * 1024);
    const unsigned r8 = lane >> 3, c32 = (lane & 7) >> 1, c16 = (lane & 1) * 16;
    const unsigned v1e = r8 * pitch + ((c32 ^ ((r8 >> 1) & 1)) * 32) + c16, v1o = v1e ^ 64;
    const unsigned vh = (lane >> 2) * 3584 + ((((lane & 3) >> 1) ^ (lane >> 5)) * 32) + c16;
    const unsigned v2 = r8 * 1024 + ((c32 ^ (r8 >> 1)) * 32) + c16;
    const unsigned cb = j * 448 + (j & 1) * 64;
    const unsigned c0 = ka ? 1024 + j * 128 : cb, c1 = ka ? 2048 + j * 128 : cb + 128, c2 = ka ? j * 128 : cb + 256;
    const unsigned ch = j * 448 + ((j & 1) ? 0 : 384);
    const int n1 = ka ? 12 : 14, nw = n1 + 18;
    auto issue = [&](int s) {
      const unsigned l0 = L_W + (s % 5) * SLOT;
      if (s < 12) {
        const unsigned sa = s < 8 ? c0 + s * 64 * pitch : c2 + (s - 8) * 128 * pitch;
        const unsigned sb_ = s < 8 ? c1 + s * 64 * pitch : sa + 64 * pitch;
        for (int p = 0; p < 16; ++p) {
          if (p == 15) vmwait<62>();
          dma(l0 + p * 1024, (p & 1) ? v1o : v1e, r1, (p < 8 ? sa : sb_) + (p & 7) * 8 * pitch);
        }
      } else if (s < n1) {
        for (int p = 0; p < 16; ++p) {
          if (p == 15) vmwait<62>();
          dma(l0 + p * 1024, vh, r1, ch + ((s - 12) * 256 + p * 16) * 3584);
        }
      } else {
        const int t = s - n1;
        for (int u = 0; u < 4; ++u) {
          int row;
          if (t < 8) row = 224 * t + (t & 1) * 32 + u * 32;
          else if (t < 12) row = 224 * (2 * (t - 8) + (u >> 1)) + (u >> 1) * 32 + 128 + (u & 1) * 32;
          else if (t < 14) row = 224 * (4 * (t - 12) + u) + ((u & 1) ? 0 : 192);
          else row = (4 * (t - 14) + u) * 32;
          const rsrc_t r = t < 14 ? r2 : ro;
          for (int p = 0; p < 4; ++p) {
            if (u == 3 && p == 3) vmwait<62>();
            dma(l0 + (u * 4 + p) * 1024, v2, r, (row + p * 8) * 1024 + nt * 128);
          }
        }
      }
    };
    dma(L_GATE, lane * 16, mkrs(p_gate, 1024), 0);
    dma(L_B1, lane * 16 + j * 448, mkrs(p_b1, 3584), 0);
    dma(L_B2, lane * 16, mkrs(p_b2, 1024), 0);
#pragma unroll 1
    for (int s = 0; s < 3; ++s) issue(s);
    vmwait<32>(); bar();
    issue(3);
    vmwait<32>(); bar();
    issue(4);
#pragma unroll 1
    for (int s = 1; s < nw - 4; ++s) {
      issue(s + 4);
      if (ka && s == 12) bar();
      vmwait<48>(); bar();
    }
    vmwait<32>(); bar();
    vmwait<16>(); bar();
    vmwait<0>(); bar();
  } else if (w == 5) {
    const rsrc_t rx = mkrs(p_x, 2048 * 1024), rws = mkrs(p_ws, WS_END);
    const unsigned vxe = (lane >> 3) * 1024 + (((lane & 7) ^ (lane >> 4)) * 16), vxo = vxe ^ 64;
    auto issue_x = [&](int s) {
      for (int p = 0; p < 16; ++p) dma(L_A + (s & 3) * SLOT + p * 1024, (p & 1) ? vxo : vxe, rx, (rt * 128 + p * 8) * 1024 + s * 128);
    };
    auto issue_a = [&](int t) {
      for (int p = 0; p < 16; ++p)
        dma_sc1(L_A + (t & 3) * SLOT + p * 1024, lane * 16, rws, WS_ACT + ((mt * 4 + (p & 3)) * 72 + 4 * t + (p >> 2)) * 1024);
    };
#pragma unroll 1
    for (int s = 0; s < 3; ++s) issue_x(s);
#pragma unroll 1
    for (int s = 0; s < 5; ++s) { vmwait<32>(); bar(); issue_x(s + 3); }
    vmwait<32>(); bar();
    vmwait<16>(); bar();
    vmwait<0>(); bar();
    const int ch1 = ka ? CH1A : CH1B, P = ka ? PA : PB;
#pragma unroll 1
    for (int i = 8; i <= ch1; ++i) bar();
#pragma unroll 1
    for (int t = 0; t < 3; ++t) issue_a(t);
#pragma unroll 1
    for (int i = ch1 + 1; i < P; ++i) bar();
#pragma unroll 1
    for (int t = 0; t < 15; ++t) { vmwait<32>(); bar(); issue_a(t + 3); }
    vmwait<32>(); bar();
    vmwait<16>(); bar();
    vmwait<0>(); bar();
  } else {
    const u64 tag = __hip_atomic_load((const GAS u64*)p_ws, __ATOMIC_RELAXED, __HIP_MEMORY_SCOPE_AGENT) + 1;
    const rsrc_t rws = mkrs(p_ws, WS_END);
    const int hp = w - 6, rt2 = mt >> 1, P = ka ? PA : PB;
    if (!ka && hp == 0) return;
    u32x4 gv[2][16], gz[2];
#pragma unroll 1
    for (int i = 0; i < P + 13; ++i) {
      if (hp == 1) {
        if (i == (ka ? CH1A : CH1B)) poll(p_ws, F_H + rt2 * 8 + (lane & 7), tag);
        if (i == P + 5) poll(p_ws, F_H + 128 + rt2 * 8 + (lane & 7), tag);
        if (i == P + 9) poll(p_ws, F_H + 256 + rt2 * 8 + (lane & 7), tag);
        if (i == P + 11) {
          poll(p_ws, F_O + rt2 * 8 + (lane & 7), tag);
          static_assert(KV_PUB >= CH1A, "epoch advance relies on this order");
          if (b == 0 && lane == 0) __hip_atomic_store((GAS u64*)p_ws, tag, __ATOMIC_RELAXED, __HIP_MEMORY_SCOPE_AGENT);
        }
        if (i == P + 12)
          *(LAS unsigned*)(size_t)(L_G64 + lane * 4) = __builtin_amdgcn_raw_buffer_load_b32(rws, lane * 4, WS_G + mt * 256, 16);
      }
      STAMP(i);
      bar();
      if (ka) {
        if (i == KV_LDS) {
          for (int hh = 0; hh < 2; ++hh) {
            const int h = 2 * hp + hh;
            f32x4 s = {0, 0, 0, 0}, z = {0, 0, 0, 0};
            for (int v = 0; v < 4; ++v) {
              s += *(const LAS f32x4*)(size_t)(L_A + (h * 4 + v) * 1024 + lane * 16);
              z += *(const LAS f32x4*)(size_t)(L_Z + (h * 4 + v) * 64 + (lane & 3) * 16);
            }
            st_sc1(__builtin_bit_cast(u32x4, s), rws, lane * 16, WS_KV + ((j * 4 + h) * 16 + rt) * 1024);
            if (lane < 4) st_sc1(__builtin_bit_cast(u32x4, z), rws, lane * 16, WS_Z + ((j * 4 + h) * 16 + rt) * 64);
          }
        }
        if (i == KV_PUB) { vmwait<0>(); set_flag(p_ws, F_KV + (j * 2 + hp) * 16 + rt, tag); }
        if (i == KV_POLL) {
          poll(p_ws, F_KV + (j * 2 + hp) * 16 + (lane & 15), tag);
          for (int hh = 0; hh < 2; ++hh) {
            int h = j * 4 + 2 * hp + hh;
            asm volatile("" : "+s"(h));
            gz[hh] = ld_sc1(rws, lane * 16, WS_Z + h * 1024);
            for (int v = 0; v < 16; ++v) gv[hh][v] = ld_sc1(rws, lane * 16 + (v & 3) * 1024, WS_KV + (h * 16 + (v & 12)) * 1024);
          }
          ++i; STAMP(i); bar();
          ++i; STAMP(i); bar();
          for (int hh = 0; hh < 2; ++hh) {
            f32x4 s = {0, 0, 0, 0}, z = __builtin_bit_cast(f32x4, gz[hh]);
            for (int v = 0; v < 16; ++v) s += __builtin_bit_cast(f32x4, gv[hh][v]);
            for (int e = 0; e < 4; ++e) {
              float t = z[e];
              t += __int_as_float(__builtin_amdgcn_update_dpp(0, __float_as_int(t), 0x124, 0xf, 0xf, false));
              t += __int_as_float(__builtin_amdgcn_update_dpp(0, __float_as_int(t), 0x128, 0xf, 0xf, false));
              float u[4];
              for (int r = 0; r < 4; ++r) u[r] = __int_as_float(__builtin_amdgcn_ds_bpermute((r * 16 + (lane >> 4)) * 4, __float_as_int(t)));
              z[e] = (u[0] + u[1]) + (u[2] + u[3]);
            }
            *(LAS u32x4*)(size_t)(L_KVB + (2 * hp + hh) * 1024 + lane * 16) = pk8(s, z);
          }
          if (hp == 0) { bar(); return; }
        }
        if (i == PO) set_flag(p_ws, F_O + rt * 8 + j, tag);
      } else {
        if (i == PH1) set_flag(p_ws, F_H + rt * 8 + j, tag);
        if (i == PH2A) set_flag(p_ws, F_H + 128 + rt * 8 + j, tag);
        if (i == PH2B) set_flag(p_ws, F_H + 256 + rt * 8 + j, tag);
      }
    }
  }
}

extern "C" void kernel_launch(void* const* d_in, const int* in_sizes, int n_in,
                              void* d_out, int out_size, void* d_ws, size_t ws_size,
                              hipStream_t stream) {
  fused_kernel<<<256, 512, 0, stream>>>((const char*)d_in[0], (const char*)d_in[1], (const char*)d_in[2],
                                        (const char*)d_in[4], (const char*)d_in[5], (const char*)d_in[6],
                                        (const char*)d_in[7], (const char*)d_in[8],
                                        (__hip_bfloat16*)d_out, (char*)d_ws);
}
```

```cpp
#include <hip/hip_runtime.h>
#include <hip/hip_bf16.h>

#define LAS __attribute__((address_space(3)))
#define GAS __attribute__((address_space(1)))
#define DEVI __device__ __forceinline__
typedef __attribute__((ext_vector_type(8))) __bf16 bf16x8;
typedef __attribute__((ext_vector_type(2))) __bf16 bf16x2;
typedef __attribute__((ext_vector_type(4))) float f32x4;
typedef __attribute__((ext_vector_type(2))) float f32x2;
typedef __attribute__((ext_vector_type(4))) short s16x4;
typedef __attribute__((ext_vector_type(4))) unsigned u32x4;
typedef __attribute__((ext_vector_type(2))) unsigned u32x2;
typedef unsigned long long u64;
typedef __amdgpu_buffer_rsrc_t rsrc_t;

constexpr int SLOT = 16384;
constexpr int L_W = 0;
constexpr int L_A = 5 * SLOT;
constexpr int L_M = 9 * SLOT;
constexpr int L_GATE = L_M, L_B1 = L_M + 1024, L_B2 = L_M + 2048, L_Z = L_M + 3072;
constexpr int L_KVB = L_M + 4096;
constexpr int L_OUT = L_M + 8192;
constexpr int L_G64 = L_GATE;
constexpr int L_TAG = L_B1 + 1008;
constexpr int L_TOTAL = 10 * SLOT;
constexpr int WS_FLAG = 512 * 1024 + 256 * 1024;
constexpr int F_H = 0;
constexpr int F_O = 1536;
constexpr int F_KV = 2048;
constexpr int WS_G = 128 * 1024;
constexpr int WS_Z = 144 * 1024;
constexpr int WS_KV = 256 * 1024;
constexpr int WS_ACT = 1024 * 1024;
constexpr int WS_END = WS_ACT + 128 * 72 * 1024;
constexpr int WS_DIAG = 16 * 1024 * 1024;

#define AH 2
#define MH1 180
#define EQ 1
#define PFD 0
#define PRW 1
#define KV_POLL 14
constexpr int PA = 13, PB = 14;
constexpr int CH1A = 12, CH1B = 13;
constexpr int TH2A = 1, TH2B = 3;
constexpr int KV_LDS = 10;
constexpr int KV_PUB = 12;
constexpr int KV_RDY = KV_POLL + 3;
constexpr int TO = KV_RDY - PA;
constexpr int LAG = 5, NBUF = 6;
constexpr int CODE_BYTES = 32768;

template <int I> struct IC { static constexpr int v = I; };
template <int B, int E, class F> DEVI void sfor(F&& f) {
  if constexpr (B < E) { f(IC<B>{}); sfor<B + 1, E>(f); }
}
DEVI void bar0() {
  __builtin_amdgcn_sched_barrier(0);
  asm volatile("" ::: "memory");
  __builtin_amdgcn_s_waitcnt(0xc07f);
  asm volatile("s_barrier" ::: "memory");
  __builtin_amdgcn_sched_barrier(0);
}
#define bar() bar0()
#define MARK(i) do {} while (0)
#define PRE() do {} while (0)
#define DUMP() do {} while (0)
#define DGP
#define DGA
template <int N> DEVI void vmwait() { asm volatile("s_waitcnt vmcnt(%0)" :: "i"(N) : "memory"); }
DEVI void sb() { __builtin_amdgcn_sched_barrier(0); }
DEVI int lane_id() { int l; asm volatile("v_mbcnt_lo_u32_b32 %0, -1, 0\n\tv_mbcnt_hi_u32_b32 %0, -1, %0" : "=v"(l)); return l; }
DEVI rsrc_t mkrs(const void* p, unsigned n) { return __builtin_amdgcn_make_buffer_rsrc((void*)p, 0, n, 0x00020000); }
DEVI void dma(unsigned lds, unsigned voff, rsrc_t rs, unsigned soff) {
  asm volatile("s_mov_b32 m0, %0\n\ts_nop 0\n\tbuffer_load_dwordx4 %1, %2, %3 offen lds" :: "s"(lds), "v"(voff), "s"(rs), "s"(soff) : "memory");
}
#define DMA4(a, b, c, d, so, fl) \
  "s_nop 0\n\tbuffer_load_dwordx4 " a ", %8, " so " offen " fl "\n\tbuffer_load_dwordx4 " b ", %8, " so " offen offset:1024 " fl \
  "\n\tbuffer_load_dwordx4 " c ", %8, " so " offen offset:2048 " fl "\n\ts_waitcnt vmcnt(62)\n\tbuffer_load_dwordx4 " d ", %8, " so " offen offset:3072 " fl "\n\t"
#define DMA16(fl) \
  asm volatile("s_mov_b32 m0, %11\n\t" DMA4("%0", "%1", "%2", "%3", "%9", fl) "s_add_u32 m0, m0, 0x1000\n\t" DMA4("%4", "%5", "%6", "%7", "%9", fl) \
               "s_add_u32 m0, m0, 0x1000\n\t" DMA4("%0", "%1", "%2", "%3", "%10", fl) "s_add_u32 m0, m0, 0x1000\n\t" DMA4("%4", "%5", "%6", "%7", "%10", fl) \
               :: "v"(v[0]), "v"(v[1]), "v"(v[2]), "v"(v[3]), "v"(v[4]), "v"(v[5]), "v"(v[6]), "v"(v[7]), "s"(rs), "s"(sa), "s"(sb_), "s"(lds) : "memory", "scc")
DEVI void dma16(unsigned lds, const unsigned (&v)[8], rsrc_t rs, unsigned sa, unsigned sb_) { DMA16("lds"); }
DEVI void dma16_sc1(unsigned lds, const unsigned (&v)[8], rsrc_t rs, unsigned sa, unsigned sb_) { DMA16("sc1 lds"); }
DEVI bf16x8 rd_frag(unsigned a) { return *(const LAS bf16x8*)(size_t)a; }
DEVI f32x4 mfma(bf16x8 a, bf16x8 b, f32x4 c) { return __builtin_amdgcn_mfma_f32_16x16x32_bf16(a, b, c, 0, 0, 0); }
DEVI void mac(f32x4& c, bf16x8 a, bf16x8 b) { asm volatile("v_mfma_f32_16x16x32_bf16 %0, %1, %2, %0" : "+v"(c) : "v"(a), "v"(b)); }
DEVI void mac0(f32x4& d, bf16x8 a, bf16x8 b) { asm volatile("v_mfma_f32_16x16x32_bf16 %0, %1, %2, 0" : "=&v"(d) : "v"(a), "v"(b)); }
DEVI void macc(f32x4& d, bf16x8 a, bf16x8 b, f32x4 c) { asm volatile("v_mfma_f32_16x16x32_bf16 %0, %1, %2, %3" : "=&v"(d) : "v"(a), "v"(b), "v"(c)); }
DEVI void settle() { asm volatile("s_nop 7\n\ts_nop 1"); sb(); }
DEVI s16x4 rd_h(unsigned a) { return __builtin_amdgcn_ds_read_tr16_b64_v4i16((LAS s16x4*)(size_t)a); }
DEVI bf16x8 cat(s16x4 lo, s16x4 hi) { return __builtin_bit_cast(bf16x8, __builtin_shufflevector(lo, hi, 0, 1, 2, 3, 4, 5, 6, 7)); }
DEVI unsigned pk(float a, float b) { f32x2 v = {a, b}; return __builtin_bit_cast(unsigned, __builtin_convertvector(v, bf16x2)); }
DEVI u32x4 pk8(f32x4 a, f32x4 b) { return u32x4{pk(a[0], a[1]), pk(a[2], a[3]), pk(b[0], b[1]), pk(b[2], b[3])}; }
DEVI f32x4 unpk4(u32x2 v) {
  return f32x4{__uint_as_float(v[0] << 16), __uint_as_float(v[0] & 0xffff0000u), __uint_as_float(v[1] << 16), __uint_as_float(v[1] & 0xffff0000u)};
}
DEVI float relu(float a) { return __int_as_float(max(__float_as_int(a), 0)); }
DEVI f32x4 relu2(f32x4 a) { f32x4 r; for (int i = 0; i < 4; ++i) r[i] = relu(a[i]); return r * r; }
constexpr float LOG2E = 1.44269504089f;
DEVI float exp2c(float a) { return __builtin_amdgcn_fmed3f(__builtin_amdgcn_exp2f(a), 0.f, 1.f); }
DEVI f32x4 phi(f32x4 a) {
  f32x4 e = a * LOG2E, r;
  for (int i = 0; i < 4; ++i) { e[i] = exp2c(e[i]); r[i] = relu(a[i]); }
  return (r + e) + 1e-6f;
}
DEVI void st_sc1(u32x4 v, rsrc_t rs, unsigned voff, unsigned soff) { __builtin_amdgcn_raw_buffer_store_b128(v, rs, voff, soff, 16); }
DEVI u32x4 ld_sc1(rsrc_t rs, unsigned voff, unsigned soff) { return __builtin_amdgcn_raw_buffer_load_b128(rs, voff, soff, 16); }
DEVI void set_flag(rsrc_t rws, unsigned l16, int idx, u32x2 tag) {
  __builtin_amdgcn_raw_buffer_store_b64(tag, rws, l16 << 20, WS_FLAG + idx * 64, 16);
}
DEVI u32x2 ld_flag(rsrc_t rws, unsigned voff, int idx) { return __builtin_amdgcn_raw_buffer_load_b64(rws, voff, WS_FLAG + idx * 64, 16); }
DEVI void poll(rsrc_t rws, unsigned voff, int idx, u32x2 tag, u32x2 v) {
#pragma unroll 1
  for (unsigned spins = 0; spins < (1u << 17); ++spins) {
    if (__all(v[0] == tag[0] && v[1] == tag[1])) break;
    asm volatile("" ::: "memory");
    v = ld_flag(rws, voff, idx);
  }
  asm volatile("" ::: "memory");
}

template <bool KA>
DEVI void compute(DGP char* p_ws, __hip_bfloat16* p_out, int w, int rt, int j, int mt, int nt) {
  const rsrc_t rws = mkrs(p_ws, WS_END);
  const int fl = (rt * 2 + (w >> 1)) * 16 + j * 2 + (w & 1);
  u32x4 pq[2][2];
  float gq[2];
  {
    constexpr int NQ = KA ? 192 : 224;
    bf16x8 xf[16][2], gf[2], one;
    s16x4 wf[NBUF][2];
    f32x2 t0, t1;
    f32x4 kv, zz;
    u32x4 hq;
    f32x4 acc[8][2], acc2[4][2], acc3[2][2], accg[2];
    u32x4 pkf[4], pvf[4];
    u32x2 bt, tg;
    f32x4 bu = {0, 0, 0, 0};
    unsigned wc[4], xb[2], bb, actv;
    {
      const int lane = lane_id(), g = lane >> 4, q = (lane & 15) >> 2, pp = lane & 3, f = (q >> 1) | ((g & 1) << 1);
      for (int c = 0; c < 4; ++c) wc[c] = L_W + (8 * g + q) * 128 + ((c ^ f) * 32) + pp * 8;
      for (int k = 0; k < 2; ++k) xb[k] = L_A + (32 * w + (lane & 15)) * 128 + (((4 * k + g) ^ ((lane & 15) >> 1)) * 16);
      bb = KA ? L_GATE + 16 * g : L_B1 + 8 * g + (j & 1) * 64;
    }
    const unsigned acts = WS_ACT + (rt * 8 + 2 * w) * (72 * 1024);
    sfor<0, NQ + LAG>([&](auto ic) {
      constexpr int n = ic.v;
      if constexpr (n < NQ) {
        constexpr int st = n >> 4, r = n & 15;
        if constexpr (r == 0) {
          bar();
          if constexpr (st == 12) {
            const int lane = lane_id(), g = lane >> 4, q = (lane & 15) >> 2, pp = lane & 3;
            for (int c = 0; c < 2; ++c) wc[c] = L_W + (st % 5) * SLOT + (8 * g + q) * 64 + ((c ^ (g & 1)) * 32) + pp * 8;
            bb = L_B1 + 8 * g + ((j & 1) ? 0 : 384);
          } else if constexpr (st > 0) {
            for (int c = 0; c < (st < 12 ? 4 : 2); ++c) { wc[c] += (st % 5) ? SLOT : -4 * SLOT; asm volatile("" : "+v"(wc[c])); }
          }
          if constexpr (!KA && st == 0)
            for (int t = 0; t < 8; ++t) {
              acc[t][0] = acc[t][1] = unpk4(*(const LAS u32x2*)(size_t)(bb + t * 32));
              asm volatile("" : "+v"(acc[t][0]), "+v"(acc[t][1]));
            }
          if constexpr (st == 12)
            for (int t = 0; t < 2; ++t) { f32x4 b = unpk4(*(const LAS u32x2*)(size_t)(bb + t * 32)); acc3[t][0] = b; acc3[t][1] = b; asm volatile("" : "+v"(acc3[t][0]), "+v"(acc3[t][1])); }
        }
      }
      sfor<0, 2>([&](auto hc) {
        constexpr int mi = hc.v, m = n - LAG;
        if constexpr (m >= 0) {
          constexpr int st = m >> 4, r = m & 15;
          const bf16x8 wv = cat(wf[m % NBUF][0], wf[m % NBUF][1]);
          if constexpr (st < 8) {
            constexpr int t = r & 7, ks = 2 * st + (r >> 3);
            if constexpr (KA && ks == 0) mac0(acc[t][mi], xf[ks][mi], wv);
            else if constexpr (KA) mac(acc[t][mi], xf[ks][mi], wv);
            else mac(acc[t][mi], wv, xf[ks][mi]);
            if constexpr (KA && t == 2) { if constexpr (ks == 0) mac0(accg[mi], gf[0], xf[0][mi]); else mac(accg[mi], gf[ks & 1], xf[ks][mi]); }
          } else if constexpr (st == 8) {
            static_assert(LAG >= 4 && LAG <= 5, "bias register hand-over");
            constexpr int ks = r & 3, t = r >> 2;
            if constexpr (ks) mac(acc2[t][mi], wv, xf[ks][mi]);
            else if constexpr (KA) mac0(acc2[t][mi], wv, xf[ks][mi]);
            else macc(acc2[t][mi], wv, xf[ks][mi], bu);
          } else if constexpr (st < 12) {
            mac(acc2[r & 3][mi], wv, xf[4 * (st - 8) + (r >> 2)][mi]);
          } else {
            mac(acc3[r & 1][mi], wv, xf[8 * (st - 12) + (r >> 1)][mi]);
          }
          sb();
        }
        if constexpr (n < NQ) {
          constexpr int st = n >> 4, r = n & 15;
          if constexpr (st < 8) {
            constexpr int kk = r >> 3, t = r & 7;
            if constexpr (t == 0) xf[2 * st + kk][mi] = rd_frag(xb[kk] + (st & 3) * SLOT + mi * 2048);
            if constexpr (KA && t == 1 && mi == 1) gf[kk] = rd_frag(bb + (2 * st + kk) * 64);
            wf[n % NBUF][mi] = rd_h(wc[t & 3] + (t >> 2) * 8192 + kk * 4096 + mi * 512);
          } else if constexpr (st == 8) {
            if constexpr (!KA && (r & 3) == 0 && mi == 0) bt = *(const LAS u32x2*)(size_t)(bb + 256 + (r >> 2) * 32);
            if constexpr (!KA && (r & 3) == 3 && mi == 0) bu = unpk4(bt);
            wf[n % NBUF][mi] = rd_h(wc[r >> 2] + (r & 3) * 4096 + mi * 512);
          } else if constexpr (st < 12) {
            wf[n % NBUF][mi] = rd_h(wc[r & 3] + (r >> 2) * 4096 + mi * 512);
          } else {
            wf[n % NBUF][mi] = rd_h(wc[r & 1] + (r >> 1) * 2048 + mi * 256);
          }
        }
        if constexpr (m == 122 && mi == 0) actv = lane_id() * 16;
        if constexpr (!KA) {
          if constexpr (m == MH1 - 4 && mi == 0) tg = *(const LAS u32x2*)(size_t)L_TAG;
          if constexpr (m == MH1 && mi == 0) { vmwait<0>(); set_flag(rws, actv, F_H + fl, tg); }
          constexpr bool e1 = m >= 124 && m < 124 + 32 / EQ, e2 = m >= 192 && m < 208;
          if constexpr (e1 || e2) {
            constexpr bool whole = e1 && EQ == 2;
            constexpr int Q = e2 ? m - 192 : whole ? 2 * (m - 124) + mi : m - 124;
            constexpr int pr = Q >> 3, ei = (Q >> 2) & 1, qq = Q & 3;
            if constexpr (whole || mi == 0) {
              const f32x4 a = e1 ? acc[2 * pr + (qq >> 1)][ei] : acc2[(2 * pr + (qq >> 1)) & 3][ei];
              t0 = f32x2{relu(a[2 * (qq & 1)]), relu(a[2 * (qq & 1) + 1])};
            }
            if constexpr (whole || mi == 1) {
              t0 *= t0;
              hq[qq] = pk(t0[0], t0[1]);
              if constexpr (qq == 3) st_sc1(hq, rws, actv, acts + (ei * 72 + (e1 ? 4 * j : 32 + 2 * j) + pr) * 1024);
            }
          }
        } else {
          if constexpr (m >= 127 && m < 131) {
            const f32x4 a = acc[m - 123][mi];
            pvf[m - 127][2 * mi] = pk(a[0], a[1]); pvf[m - 127][2 * mi + 1] = pk(a[2], a[3]);
          }
          if constexpr (m == 126) asm volatile("" :: "v"(accg[mi]));
          constexpr int S = 2 * m + mi - 248;
          if constexpr (S >= 0 && S < 48) {
            constexpr int U = S / 3, k = S % 3, h = U >> 2;
            const f32x4 a4 = acc[h][(U >> 1) & 1];
            const f32x2 a = {a4[2 * (U & 1)], a4[2 * (U & 1) + 1]};
            if constexpr (k == 0) { t0 = a * LOG2E; t0[0] = exp2c(t0[0]); }
            if constexpr (k == 1) { t0[1] = exp2c(t0[1]); t1 = f32x2{relu(a[0]), relu(a[1])}; }
            if constexpr (k == 2) { t0 = (t1 + t0) + 1e-6f; pkf[h][U & 3] = pk(t0[0], t0[1]); }
          }
          if constexpr (S >= 12 && S <= 48 && S % 12 == 0) {
            constexpr int h = S / 12 - 1;
            const bf16x8 kf = __builtin_bit_cast(bf16x8, pkf[h]);
            sb();
            mac0(kv, kf, __builtin_bit_cast(bf16x8, pvf[h]));
            mac0(zz, kf, one);
          }
          if constexpr (S >= 18 && S <= 54 && S % 12 == 6) {
            constexpr int h = S / 12 - 1;
            *(LAS f32x4*)(size_t)(L_A + (h * 4 + w) * 1024 + actv) = kv;
            *(LAS f32x4*)(size_t)(bb + (L_Z - L_GATE) + (h * 4 + w) * 64) = zz;
          }
          if constexpr (S == 8) { one = __builtin_bit_cast(bf16x8, u32x4{0x3f803f80u, 0x3f803f80u, 0x3f803f80u, 0x3f803f80u}); asm volatile("" : "+v"(one)); }
        }
        sb();
      });
    });
    settle();
    if constexpr (KA) {
      bar();
      for (int mi = 0; mi < 2; ++mi) {
        gq[mi] = __builtin_amdgcn_rcpf(1.f + __expf(-accg[mi][0]));
        for (int hp = 0; hp < 2; ++hp) pq[mi][hp] = pk8(phi(acc2[2 * hp][mi]), phi(acc2[2 * hp + 1][mi]));
      }
    } else {
      for (int mi = 0; mi < 2; ++mi)
        st_sc1(pk8(relu2(acc3[0][mi]), relu2(acc3[1][mi])), rws, actv, acts + (mi * 72 + 48 + j) * 1024);
    }
  }
  const int lane = lane_id(), g = lane >> 4, q = (lane & 15) >> 2, pp = lane & 3, f = (q >> 1) | ((g & 1) << 1);
  const int wm = w & 1, wn = w >> 1;
  f32x4 o[2][2];
  for (int t = 0; t < 2; ++t) { f32x4 b = unpk4(*(const LAS u32x2*)(size_t)(L_B2 + nt * 128 + wn * 64 + t * 32 + g * 8)); o[t][0] = b; o[t][1] = b; asm volatile("" : "+v"(o[t][0]), "+v"(o[t][1])); }
  const u32x2 tag = *(const LAS u32x2*)(size_t)L_TAG;
  unsigned wa[2], aa = L_A + wm * 8192 + lane * 16, wslot = KA ? 2 : 4;
  for (int c = 0; c < 2; ++c) wa[c] = L_W + wslot * SLOT + (4 * g + q) * 128 + (((2 * wn + c) ^ f) * 32) + pp * 8;
  bf16x8 af[2][4][2];
  s16x4 bf[2][4][2][2];
  auto stage2 = [&](auto sc, auto rdc, auto mmc) {
    constexpr int s = sc.v;
    if constexpr (rdc.v) bar();
    sfor<0, 16>([&](auto kc) {
      constexpr int u = kc.v >> 2, c = (kc.v >> 1) & 1, mi = kc.v & 1;
      if constexpr (mmc.v) { mac(o[c][mi], cat(bf[1 - s][u][c][0], bf[1 - s][u][c][1]), af[1 - s][u][mi]); sb(); }
      if constexpr (rdc.v && kc.v < 12) {
        sfor<2 * kc.v, 2 * kc.v + 2>([&](auto rc) {
          constexpr int ru = rc.v / 6, k = rc.v % 6;
          if constexpr (k < 2) af[s][ru][k] = rd_frag(aa + k * 4096 + ru * 1024);
          else bf[s][ru][(k - 2) >> 1][k & 1] = rd_h(wa[(k - 2) >> 1] + ru * 4096 + (k & 1) * 2048);
        });
      }
      sb();
    });
    if constexpr (rdc.v) {
      const int d = wslot == 4 ? -4 * SLOT : SLOT;
      wslot = wslot == 4 ? 0 : wslot + 1;
      wa[0] += d; wa[1] += d;
      aa = L_A + ((aa - L_A + SLOT) & (4 * SLOT - 1));
      asm volatile("" : "+v"(wa[0]), "+v"(wa[1]), "+v"(aa));
    }
  };
  static_assert((TH2A & TH2B & ~TO & 1) == 1 && TO >= 2 && TO + 2 <= 10, "hooks sit behind fixed halves of the loop");
  stage2(IC<0>{}, IC<1>{}, IC<0>{});
#pragma unroll 1
  for (int t = 1; t < 17; t += 2) {
    if (t == 15) {
      settle();
      for (int mi = 0; mi < 2; ++mi) {
        const float s = 1.f - *(const LAS float*)(size_t)(L_G64 + (wm * 32 + mi * 16 + (lane & 15)) * 4);
        o[0][mi] *= s; o[1][mi] *= s;
      }
    }
    stage2(IC<1>{}, IC<1>{}, IC<1>{});
    if (!KA && t == TH2A) { vmwait<2>(); set_flag(rws, lane * 16, F_H + 512 + fl, tag); }
    if (!KA && t == TH2B) { vmwait<1>(); set_flag(rws, lane * 16, F_H + 1024 + fl, tag); }
    stage2(IC<0>{}, IC<1>{}, IC<1>{});
    if (KA && t + 1 == TO + 2) { vmwait<0>(); set_flag(rws, lane * 16, F_O + fl, tag); }
    if (KA && t + 1 == TO) {
      const unsigned acts = WS_ACT + (rt * 8 + 2 * w) * (72 * 1024);
      for (int hp = 0; hp < 2; ++hp) {
        const u32x4 k0 = *(const LAS u32x4*)(size_t)(L_KVB + (2 * hp) * 1024 + lane * 16);
        const u32x4 k1 = *(const LAS u32x4*)(size_t)(L_KVB + (2 * hp + 1) * 1024 + lane * 16);
        const bf16x8 kv0 = __builtin_bit_cast(bf16x8, u32x4{k0[0], k0[1], 0, 0}), z0 = __builtin_bit_cast(bf16x8, u32x4{k0[2], k0[3], 0, 0});
        const bf16x8 kv1 = __builtin_bit_cast(bf16x8, u32x4{0, 0, k1[0], k1[1]}), z1 = __builtin_bit_cast(bf16x8, u32x4{0, 0, k1[2], k1[3]});
        for (int mi = 0; mi < 2; ++mi) {
          const bf16x8 pf = __builtin_bit_cast(bf16x8, pq[mi][hp]);
          const f32x4 zero = {0, 0, 0, 0};
          f32x4 n0 = mfma(kv0, pf, zero), d0 = mfma(z0, pf, zero), n1 = mfma(kv1, pf, zero), d1 = mfma(z1, pf, zero);
          n0 *= gq[mi] * __builtin_amdgcn_rcpf(d0[0] + 1e-6f);
          n1 *= gq[mi] * __builtin_amdgcn_rcpf(d1[0] + 1e-6f);
          st_sc1(pk8(n0, n1), rws, lane * 16, acts + (mi * 72 + 56 + 2 * j + hp) * 1024);
        }
      }
      __builtin_amdgcn_raw_buffer_store_b32(__float_as_uint(lane < 16 ? gq[0] : gq[1]), rws, (j == 0 && lane < 32) ? lane * 4 : WS_END,
                                            WS_G + (rt * 128 + 32 * w) * 4, 16);
    }
  }
  stage2(IC<1>{}, IC<1>{}, IC<1>{});
  stage2(IC<0>{}, IC<0>{}, IC<1>{});
  settle();
  const unsigned ob = L_OUT + w * 2048;
  for (int mi = 0; mi < 2; ++mi)
    for (int c = 0; c < 2; ++c) {
      const int row = mi * 16 + (lane & 15);
      *(LAS u32x2*)(size_t)(ob + row * 64 + (((2 * c + (g >> 1)) ^ ((row >> 1) & 3)) * 16) + (g & 1) * 8) =
          u32x2{pk(o[c][mi][0], o[c][mi][1]), pk(o[c][mi][2], o[c][mi][3])};
    }
  for (int h = 0; h < 2; ++h) {
    const int row = h * 16 + (lane >> 2);
    const u32x4 v = *(const LAS u32x4*)(size_t)(ob + row * 64 + (((lane & 3) ^ ((row >> 1) & 3)) * 16));
    *(u32x4*)((char*)p_out + (size_t)(mt * 64 + wm * 32 + row) * 1024 + nt * 128 + wn * 64 + (lane & 3) * 16) = v;
  }
  DUMP();
}

__global__ void __launch_bounds__(512, 2)
fused_kernel(const char* p_x, const char* p_wqkv, const char* p_wo, const char* p_w1, const char* p_b1,
             const char* p_w2, const char* p_b2, const char* p_gate, __hip_bfloat16* p_out, char* p_ws) {
  __shared__ __attribute__((aligned(16))) char smem[L_TOTAL];
  asm volatile("" :: "v"((LAS char*)smem) : "memory");
  const char* code;
  asm volatile("s_getpc_b64 %0" : "=s"(code));
  const int lane = threadIdx.x & 63, w = __builtin_amdgcn_readfirstlane(threadIdx.x >> 6);
  const int b = blockIdx.x, xcd = b & 7, bi = b >> 3;
  const int rt = (xcd & 1) * 8 + (bi & 7), j = (xcd >> 1) * 2 + (bi >> 4);
  const bool ka = ((bi >> 3) & 1) == 0;
  const int mt = xcd * 4 + (bi & 3), nt = bi >> 2;
  if (w < 4) {
    if (ka) compute<true>(DGA p_ws, p_out, w, rt, j, mt, nt);
    else compute<false>(DGA p_ws, p_out, w, rt, j, mt, nt);
  } else if (w == 4) {
    const unsigned pitch = ka ? 3072 : 3584;
    rsrc_t rs = mkrs(ka ? p_wqkv : p_w1, 512 * pitch);
    const unsigned r8 = lane >> 3, c32 = (lane & 7) >> 1, c16 = (lane & 1) * 16;
    unsigned v[8], slot = L_W;
    for (int p = 0; p < 8; ++p) v[p] = (r8 + 8 * p) * pitch + (((c32 ^ ((r8 >> 1) & 1)) * 32) ^ ((p & 1) * 64)) + c16 - (p & 3) * 1024;
    const unsigned cb = j * 448 + (j & 1) * 64;
    unsigned sa = ka ? 1024 + j * 128 : cb, sd = ka ? 1024 : 128, step = 64 * pitch;
    auto issue = [&]() {
      dma16(slot, v, rs, sa, sa + sd);
      slot = slot == L_W + 4 * SLOT ? L_W : slot + SLOT;
      sa += step;
    };
    __builtin_amdgcn_s_setprio(PRW);
#pragma unroll 1
    for (int s = 0; s < 8; ++s) {
      if (s >= AH) { PRE(); vmwait<16 * (AH - 1)>(); bar(); }
      issue();
    }
    sa = ka ? j * 128 : cb + 256; sd = 64 * pitch; step = 128 * pitch;
    if (AH == 2) {
      vmwait<16>(); bar(); issue(); issue();
      vmwait<32>(); bar(); issue(); issue();
    } else {
      vmwait<32>(); bar(); issue(); issue();
      vmwait<48>(); bar(); issue();
      vmwait<48>(); bar(); issue();
    }
    if (!ka) {
      for (int p = 0; p < 8; ++p) v[p] = ((lane >> 2) + 16 * p) * 3584 + ((((lane & 3) >> 1) ^ (lane >> 5)) * 32) + c16 - (p & 3) * 1024;
      sa = j * 448 + ((j & 1) ? 0 : 384); sd = 128 * 3584; step = 256 * 3584;
#pragma unroll 1
      for (int s = 0; s < 2; ++s) { PRE(); vmwait<48>(); bar(); issue(); }
    }
    for (int p = 0; p < 8; ++p) v[p] = (r8 + 8 * p) * 1024 + ((c32 ^ (r8 >> 1)) * 32) + c16 + nt * 128 - (p & 3) * 1024;
    rs = mkrs(p_w2, 1792 * 1024);
    sa = 0; sd = 64 * 1024; step = 256 * 1024;
    unsigned alt = 448 * 1024;
#pragma unroll 1
    for (int seg = 0; seg < 5; ++seg) {
      int n = 5;
      if (seg == 1) { n = 3; if (ka) bar(); }
      if (seg == 2) { n = 4; sa = 128 * 1024; sd = 256 * 1024; step = 448 * 1024; alt = 2 * step; }
      if (seg == 3) { n = 2; sa = 192 * 1024; sd = 448 * 1024; step = 896 * 1024; alt = 2 * step; }
      if (seg == 4) { n = 4; sa = 0; sd = 64 * 1024; step = 128 * 1024; alt = 2 * step; rs = mkrs(p_wo, 512 * 1024); }
#pragma unroll 1
      do {
        PRE(); vmwait<48>(); bar();
        issue();
        step = alt - step;
      } while (--n);
    }
    vmwait<48>(); bar();
    vmwait<32>(); bar();
    vmwait<16>(); bar();
    vmwait<0>(); bar();
    DUMP();
  } else if (w == 5) {
    unsigned v[8];
    for (int p = 0; p < 8; ++p) v[p] = ((lane >> 3) + 8 * p) * 1024 + ((((lane & 7) ^ (lane >> 4)) * 16) ^ ((p & 1) * 64)) - (p & 3) * 1024;
    const rsrc_t rx = mkrs(p_x, 2048 * 1024), rws = mkrs(p_ws, WS_END);
#pragma unroll 1
    for (int s = 0; s < 8; ++s) {
      if (s >= AH) { PRE(); vmwait<16 * (AH - 1)>(); bar(); }
      dma16(L_A + (s & 3) * SLOT, v, rx, rt * 131072 + s * 128, rt * 131072 + s * 128 + 65536);
    }
    if (AH == 3) { vmwait<32>(); bar(); }
    vmwait<16>(); bar();
    vmwait<0>(); bar();
    for (int p = 0; p < 8; ++p) v[p] = lane * 16 + (p >> 2) * (72 * 1024);
#pragma unroll 1
    for (int i = ka ? CH1A : CH1B; i >= 8; --i) bar();
    static_assert(CH1A + 1 == PA && CH1B + 1 == PB, "no barrier between the poll and phase 2");
    unsigned sa = WS_ACT + mt * 4 * 72 * 1024;
#pragma unroll 1
    for (int t = 0; t < 4; ++t, sa += 4096) {
      if (t == 2) { PRE(); vmwait<16>(); bar(); }
      dma16_sc1(L_A + t * SLOT, v, rws, sa, sa + 144 * 1024);
    }
#pragma unroll 1
    for (int t = 4; t < 18; ++t, sa += 4096) {
      const unsigned l = L_A + (t & 3) * SLOT, s2 = sa + 144 * 1024;
      PRE(); vmwait<32>(); bar();
      dma16_sc1(l, v, rws, sa, s2);
    }
    vmwait<32>(); bar();
    vmwait<16>(); bar();
    vmwait<0>(); bar();
    DUMP();
  } else {
    const rsrc_t rws = mkrs(p_ws, WS_END);
    const int hp = w - 6;
    if (hp == 0) {
      dma(ka ? L_GATE : L_B2, lane * 16, mkrs(ka ? p_gate : p_b2, 1024), 0);
      dma(ka ? L_B2 : L_B1, ka ? lane * 16 : lane < 28 ? lane * 16 + j * 448 : 4096, mkrs(ka ? p_b2 : p_b1, ka ? 1024 : 3584), 0);
      if (!ka) { vmwait<0>(); return; }
    }
    const u64 tag64 = __hip_atomic_load((const GAS u64*)p_ws, __ATOMIC_RELAXED, __HIP_MEMORY_SCOPE_AGENT) + 1;
    const u32x2 tag = __builtin_bit_cast(u32x2, tag64), none = ~tag;
    unsigned pf[6] = {};
    if (hp) {
      pf[0] = __builtin_amdgcn_raw_buffer_load_b32(mkrs(code, CODE_BYTES), ((bi % (CODE_BYTES / 8192)) * 64 + lane) * 128, 0, 0);
      sb();
      __builtin_amdgcn_s_sleep(PFD);
      pf[1] = __builtin_amdgcn_raw_buffer_load_b32(mkrs(p_x, 2048 * 1024), lane * 128, (b & 1) * 1048576 + (b >> 1) * 8192, 0);
      pf[2] = __builtin_amdgcn_raw_buffer_load_b32(mkrs(p_wqkv + b * 6144, 6144), lane * 128, 0, 0);
      pf[3] = __builtin_amdgcn_raw_buffer_load_b32(mkrs(p_w1 + b * 7168, 7168), lane * 128, 0, 0);
      pf[4] = __builtin_amdgcn_raw_buffer_load_b32(mkrs(p_w2 + b * 7168, 7168), lane * 128, 0, 0);
      pf[5] = __builtin_amdgcn_raw_buffer_load_b32(mkrs(p_wo + b * 2048, 2048), lane * 128, 0, 0);
    } else {
      vmwait<0>();
    }
    const unsigned fo = (lane & 15) * 64;
    auto bars = [&](int n) {
#pragma unroll 1
      for (; n > 0; --n) bar();
    };
    bar();
    bars(6);
    *(LAS u32x2*)(size_t)L_TAG = tag;
    asm volatile("" :: "v"(pf[0]), "v"(pf[1]), "v"(pf[2]), "v"(pf[3]), "v"(pf[4]), "v"(pf[5]));
    if (ka) {
      bars(KV_LDS - 6);
      {
        f32x4 p[3][4];
        for (int v = 0; v < 4; ++v) {
          for (int hh = 0; hh < 2; ++hh) p[hh][v] = *(const LAS f32x4*)(size_t)(L_A + ((2 * hp + hh) * 4 + v) * 1024 + lane * 16);
          p[2][v] = *(const LAS f32x4*)(size_t)(L_Z + (hp * 8 + (lane & 4) + v) * 64 + (lane & 3) * 16);
        }
        sb();
        for (int k = 0; k < 3; ++k) p[k][0] = (p[k][0] + p[k][1]) + (p[k][2] + p[k][3]);
        st_sc1(pk8(p[0][0], p[1][0]), rws, lane * 16, WS_KV + ((j * 2 + hp) * 16 + rt) * 1024);
        st_sc1(__builtin_bit_cast(u32x4, p[2][0]), rws, lane < 8 ? (lane & 4) * 256 + (lane & 3) * 16 : WS_END, WS_Z + (j * 2 + hp) * 2048 + rt * 64);
      }
      bars(CH1A - KV_LDS - 1);
      vmwait<0>();
      set_flag(rws, lane * 16, F_KV + (j * 2 + hp) * 16 + rt, tag);
      if (hp) poll(rws, fo, F_H + mt * 16, tag, none);
      bars(KV_POLL - CH1A);
      const u32x2 early = ld_flag(rws, fo, F_KV + (j * 2 + hp) * 16);
      bar();
      poll(rws, fo, F_KV + (j * 2 + hp) * 16, tag, early);
      u32x4 gv[16], gz[2];
      for (int hh = 0; hh < 2; ++hh) gz[hh] = ld_sc1(rws, lane * 16 + hh * 1024, WS_Z + (j * 2 + hp) * 2048);
      for (int v = 0; v < 16; ++v) gv[v] = ld_sc1(rws, lane * 16 + (v & 3) * 1024, WS_KV + ((j * 2 + hp) * 16 + (v & 12)) * 1024);
      const u32x2 e2 = ld_flag(rws, fo, F_H + 512 + mt * 16);
      bar();
      bar();
      for (int hh = 0; hh < 2; ++hh) {
        const unsigned e = (lane >> 4) == ((lane & 15) >> 2) ? ((lane & 1) ? 0x3f800000u : 0x3f80u) : 0u, e0 = (lane & 2) ? 0u : e, e1 = (lane & 2) ? e : 0u;
        const bf16x8 sel = __builtin_bit_cast(bf16x8, hh ? u32x4{0, 0, e0, e1} : u32x4{e0, e1, 0, 0});
        f32x4 s = {0, 0, 0, 0}, z = __builtin_bit_cast(f32x4, gz[hh]);
        for (int v = 0; v < 16; ++v) s = mfma(sel, __builtin_bit_cast(bf16x8, gv[v]), s);
        for (int e = 0; e < 4; ++e) {
          float t = z[e];
          t += __int_as_float(__builtin_amdgcn_update_dpp(0, __float_as_int(t), 0x124, 0xf, 0xf, false));
          t += __int_as_float(__builtin_amdgcn_update_dpp(0, __float_as_int(t), 0x128, 0xf, 0xf, false));
          float u[4];
          for (int r = 0; r < 4; ++r) u[r] = __int_as_float(__builtin_amdgcn_ds_bpermute((r * 16 + (lane >> 4)) * 4, __float_as_int(t)));
          z[e] = (u[0] + u[1]) + (u[2] + u[3]);
        }
        *(LAS u32x4*)(size_t)(L_KVB + (2 * hp + hh) * 1024 + lane * 16) = pk8(s, z);
      }
      static_assert(KV_RDY <= PA + 5, "wave 7 has to be back for the second h poll");
      if (hp == 0) { bar(); DUMP(); return; }
      u32x2 e3 = e2;
      if (KV_RDY < PA + 5) { e3 = ld_flag(rws, fo, F_H + 512 + mt * 16); bars(PA + 5 - KV_RDY); }
      poll(rws, fo, F_H + 512 + mt * 16, tag, e3);
    } else {
      bars(CH1B - 7);
      poll(rws, fo, F_H + mt * 16, tag, none);
      bars(PB + 3 - CH1B);
      const u32x2 e2 = ld_flag(rws, fo, F_H + 512 + mt * 16);
      bars(2);
      poll(rws, fo, F_H + 512 + mt * 16, tag, e2);
    }
    bars(2);
    u32x2 e = ld_flag(rws, fo, F_H + 1024 + mt * 16);
    bars(2);
    poll(rws, fo, F_H + 1024 + mt * 16, tag, e);
    e = ld_flag(rws, fo, F_O + mt * 16);
    bars(2);
    poll(rws, fo, F_O + mt * 16, tag, e);
    const unsigned gval = __builtin_amdgcn_raw_buffer_load_b32(rws, lane * 4, WS_G + mt * 256, 16);
    u32x2 all[2];
    if (b == 0) for (int k = 0; k < 2; ++k) all[k] = ld_flag(rws, (lane >> 3) * 2048 + (lane & 7) * 128, F_H + k * 256);
    bars(2);
    *(LAS unsigned*)(size_t)(L_G64 + lane * 4) = gval;
    bar();
    if (b == 0) {
      for (int k = 0; k < 2; ++k) poll(rws, (lane >> 3) * 2048 + (lane & 7) * 128, F_H + k * 256, tag, all[k]);
      __builtin_amdgcn_raw_buffer_store_b64(tag, rws, lane << 24, 0, 16);
    }
    DUMP();
  }
}

extern "C" void kernel_launch(void* const* d_in, const int* in_sizes, int n_in,
                              void* d_out, int out_size, void* d_ws, size_t ws_size,
                              hipStream_t stream) {
  fused_kernel<<<256, 512, 0, stream>>>((const char*)d_in[0], (const char*)d_in[1], (const char*)d_in[2],
                                        (const char*)d_in[4], (const char*)d_in[5], (const char*)d_in[6],
                                        (const char*)d_in[7], (const char*)d_in[8],
                                        (__hip_bfloat16*)d_out, (char*)d_ws);
}
```

```cpp
#include <hip/hip_runtime.h>
#include <hip/hip_bf16.h>

#define LAS __attribute__((address_space(3)))
#define GAS __attribute__((address_space(1)))
#define DEVI __device__ __forceinline__
typedef __attribute__((ext_vector_type(8))) __bf16 bf16x8;
typedef __attribute__((ext_vector_type(2))) __bf16 bf16x2;
typedef __attribute__((ext_vector_type(4))) float f32x4;
typedef __attribute__((ext_vector_type(2))) float f32x2;
typedef __attribute__((ext_vector_type(4))) short s16x4;
typedef __attribute__((ext_vector_type(4))) unsigned u32x4;
typedef __attribute__((ext_vector_type(2))) unsigned u32x2;
typedef unsigned long long u64;
typedef __amdgpu_buffer_rsrc_t rsrc_t;

constexpr int SLOT = 16384;
constexpr int L_W = 0;
constexpr int L_A = 5 * SLOT;
constexpr int L_M = 9 * SLOT;
constexpr int L_GATE = L_M, L_B1 = L_M + 1024, L_B2 = L_M + 2048, L_Z = L_M + 3072;
constexpr int L_KVB = L_M + 4096;
constexpr int L_OUT = L_M + 8192;
constexpr int L_G64 = L_GATE;
constexpr int L_TAG = L_B1 + 1008;
constexpr int L_TOTAL = 10 * SLOT;
constexpr int WS_FLAG = 512 * 1024 + 256 * 1024;
constexpr int F_H = 0;
constexpr int F_O = 1536;
constexpr int F_KV = 2048;
constexpr int WS_G = 128 * 1024;
constexpr int WS_Z = 144 * 1024;
constexpr int WS_KV = 256 * 1024;
constexpr int WS_ACT = 1024 * 1024;
constexpr int WS_END = WS_ACT + 128 * 72 * 1024;
constexpr int WS_DIAG = 16 * 1024 * 1024;

#define AH 2
#define MH1 180
#define EQ 1
#define PF 5
#define PFB 0
#define NTW 0
#define WSTAIL 0
#define PRW 1
#define KV_POLL 14
constexpr int PA = 13, PB = 14;
constexpr int CH1A = 12, CH1B = 13;
constexpr int TH2A = 1, TH2B = 3;
constexpr int KV_LDS = 10;
constexpr int KV_PUB = 12;
constexpr int KV_RDY = KV_POLL + 3;
constexpr int TO = KV_RDY - PA;
constexpr int LAG = 5, NBUF = 6;
constexpr int CODE_BYTES = 34816;

template <int I> struct IC { static constexpr int v = I; };
template <int B, int E, class F> DEVI void sfor(F&& f) {
  if constexpr (B < E) { f(IC<B>{}); sfor<B + 1, E>(f); }
}
DEVI void bar0() {
  __builtin_amdgcn_sched_barrier(0);
  asm volatile("" ::: "memory");
  __builtin_amdgcn_s_waitcnt(0xc07f);
  asm volatile("s_barrier" ::: "memory");
  __builtin_amdgcn_sched_barrier(0);
}
#define bar() bar0()
#define MARK(i) do {} while (0)
#define PRE() do {} while (0)
#define DUMP() do {} while (0)
#define DGP
#define DGA
template <int N> DEVI void vmwait() { asm volatile("s_waitcnt vmcnt(%0)" :: "i"(N) : "memory"); }
DEVI void sb() { __builtin_amdgcn_sched_barrier(0); }
DEVI int lane_id() { int l; asm volatile("v_mbcnt_lo_u32_b32 %0, -1, 0\n\tv_mbcnt_hi_u32_b32 %0, -1, %0" : "=v"(l)); return l; }
DEVI rsrc_t mkrs(const void* p, unsigned n) { return __builtin_amdgcn_make_buffer_rsrc((void*)p, 0, n, 0x00020000); }
DEVI void dma(unsigned lds, unsigned voff, rsrc_t rs, unsigned soff) {
  asm volatile("s_mov_b32 m0, %0\n\ts_nop 0\n\tbuffer_load_dwordx4 %1, %2, %3 offen lds" :: "s"(lds), "v"(voff), "s"(rs), "s"(soff) : "memory");
}
DEVI void touch(unsigned voff, rsrc_t rs, unsigned soff) {
  asm volatile("s_mov_b32 m0, %0\n\ts_nop 0\n\tbuffer_load_dword %1, %2, %3 offen lds" :: "s"(L_OUT), "v"(voff), "s"(rs), "s"(soff) : "memory");
}
#define DMA4(a, b, c, d, so, fl) \
  "s_nop 0\n\tbuffer_load_dwordx4 " a ", %8, " so " offen " fl "\n\tbuffer_load_dwordx4 " b ", %8, " so " offen offset:1024 " fl \
  "\n\tbuffer_load_dwordx4 " c ", %8, " so " offen offset:2048 " fl "\n\ts_waitcnt vmcnt(62)\n\tbuffer_load_dwordx4 " d ", %8, " so " offen offset:3072 " fl "\n\t"
#define DMA16(fl) \
  asm volatile("s_mov_b32 m0, %11\n\t" DMA4("%0", "%1", "%2", "%3", "%9", fl) "s_add_u32 m0, m0, 0x1000\n\t" DMA4("%4", "%5", "%6", "%7", "%9", fl) \
               "s_add_u32 m0, m0, 0x1000\n\t" DMA4("%0", "%1", "%2", "%3", "%10", fl) "s_add_u32 m0, m0, 0x1000\n\t" DMA4("%4", "%5", "%6", "%7", "%10", fl) \
               :: "v"(v[0]), "v"(v[1]), "v"(v[2]), "v"(v[3]), "v"(v[4]), "v"(v[5]), "v"(v[6]), "v"(v[7]), "s"(rs), "s"(sa), "s"(sb_), "s"(lds) : "memory", "scc")
DEVI void dma16(unsigned lds, const unsigned (&v)[8], rsrc_t rs, unsigned sa, unsigned sb_) { DMA16("lds"); }
DEVI void dma16_nt(unsigned lds, const unsigned (&v)[8], rsrc_t rs, unsigned sa, unsigned sb_) { DMA16("nt lds"); }
DEVI void dma16_sc1(unsigned lds, const unsigned (&v)[8], rsrc_t rs, unsigned sa, unsigned sb_) { DMA16("sc1 lds"); }
DEVI bf16x8 rd_frag(unsigned a) { return *(const LAS bf16x8*)(size_t)a; }
DEVI f32x4 mfma(bf16x8 a, bf16x8 b, f32x4 c) { return __builtin_amdgcn_mfma_f32_16x16x32_bf16(a, b, c, 0, 0, 0); }
DEVI void mac(f32x4& c, bf16x8 a, bf16x8 b) { asm volatile("v_mfma_f32_16x16x32_bf16 %0, %1, %2, %0" : "+v"(c) : "v"(a), "v"(b)); }
DEVI void mac0(f32x4& d, bf16x8 a, bf16x8 b) { asm volatile("v_mfma_f32_16x16x32_bf16 %0, %1, %2, 0" : "=&v"(d) : "v"(a), "v"(b)); }
DEVI void mac1(f32x4& d, bf16x8 a, bf16x8 b) { asm volatile("v_mfma_f32_16x16x32_bf16 %0, %1, %2, 1.0" : "=&v"(d) : "v"(a), "v"(b)); }
DEVI void macc(f32x4& d, bf16x8 a, bf16x8 b, f32x4 c) { asm volatile("v_mfma_f32_16x16x32_bf16 %0, %1, %2, %3" : "=&v"(d) : "v"(a), "v"(b), "v"(c)); }
DEVI void settle() { asm volatile("s_nop 7\n\ts_nop 1"); sb(); }
DEVI s16x4 rd_h(unsigned a) { return __builtin_amdgcn_ds_read_tr16_b64_v4i16((LAS s16x4*)(size_t)a); }
DEVI bf16x8 cat(s16x4 lo, s16x4 hi) { return __builtin_bit_cast(bf16x8, __builtin_shufflevector(lo, hi, 0, 1, 2, 3, 4, 5, 6, 7)); }
DEVI unsigned pk(float a, float b) { f32x2 v = {a, b}; return __builtin_bit_cast(unsigned, __builtin_convertvector(v, bf16x2)); }
DEVI u32x4 pk8(f32x4 a, f32x4 b) { return u32x4{pk(a[0], a[1]), pk(a[2], a[3]), pk(b[0], b[1]), pk(b[2], b[3])}; }
DEVI f32x4 unpk4(u32x2 v) {
  return f32x4{__uint_as_float(v[0] << 16), __uint_as_float(v[0] & 0xffff0000u), __uint_as_float(v[1] << 16), __uint_as_float(v[1] & 0xffff0000u)};
}
DEVI float ssq(float a) { float r; asm("v_mul_f32_e64 %0, %1, |%1|" : "=v"(r) : "v"(a)); return r; }
DEVI unsigned relu_pk(unsigned a) { unsigned r; asm("v_pk_max_i16 %0, %1, 0" : "=v"(r) : "v"(a)); return r; }
DEVI float fma1(float a, float l) { float r; asm("v_fma_f32 %0, %1, %2, -%2" : "=v"(r) : "v"(a), "s"(l)); return r; }
DEVI float maxadd(float a, float e, float c) { float r; asm("v_max_f32 %0, %1, %2\n\tv_add_f32 %0, %3, %0" : "=v"(r) : "v"(a), "v"(e), "s"(c)); return r; }
DEVI float relu(float a) { return __int_as_float(max(__float_as_int(a), 0)); }
DEVI f32x4 relu2(f32x4 a) { f32x4 r; for (int i = 0; i < 4; ++i) r[i] = relu(a[i]); return r * r; }
constexpr float LOG2E = 1.44269504089f;
DEVI float exp2c(float a) { return __builtin_amdgcn_fmed3f(__builtin_amdgcn_exp2f(a), 0.f, 1.f); }
DEVI f32x4 phi(f32x4 a1) {
  f32x4 e = a1 * LOG2E - LOG2E;
  for (int i = 0; i < 4; ++i) e[i] = __builtin_amdgcn_fmed3f(a1[i], exp2c(e[i]), __builtin_inff());
  return e + 1e-6f;
}
DEVI void st_sc1(u32x4 v, rsrc_t rs, unsigned voff, unsigned soff) { __builtin_amdgcn_raw_buffer_store_b128(v, rs, voff, soff, 16); }
DEVI u32x4 ld_sc1(rsrc_t rs, unsigned voff, unsigned soff) { return __builtin_amdgcn_raw_buffer_load_b128(rs, voff, soff, 16); }
DEVI void set_flag(rsrc_t rws, unsigned l16, int idx, u32x2 tag) {
  __builtin_amdgcn_raw_buffer_store_b64(tag, rws, l16 << 20, WS_FLAG + idx * 64, 16);
}
DEVI u32x2 ld_flag(rsrc_t rws, unsigned voff, int idx) { return __builtin_amdgcn_raw_buffer_load_b64(rws, voff, WS_FLAG + idx * 64, 16); }
DEVI void poll(rsrc_t rws, unsigned voff, int idx, u32x2 tag, u32x2 v) {
#pragma unroll 1
  for (unsigned spins = 0; spins < (1u << 17); ++spins) {
    if (__all(v[0] == tag[0] && v[1] == tag[1])) break;
    asm volatile("" ::: "memory");
    v = ld_flag(rws, voff, idx);
  }
  asm volatile("" ::: "memory");
}

template <bool KA>
DEVI void compute(DGP char* p_ws, __hip_bfloat16* p_out, int w, int rt, int j, int mt, int nt) {
  const rsrc_t rws = mkrs(p_ws, WS_END);
  const int fl = (rt * 2 + (w >> 1)) * 16 + j * 2 + (w & 1);
  u32x4 pq[2][2];
  float gq[2];
  {
    constexpr int NQ = KA ? 192 : 224;
    bf16x8 xf[16][2], gf[2], one;
    s16x4 wf[NBUF][2];
    f32x2 t0, t1;
    f32x4 kv, zz;
    u32x4 hq;
    f32x4 acc[8][2], acc2[4][2], acc3[2][2], accg[2];
    u32x4 pkf[4], pvf[4];
    u32x2 bt, tg;
    f32x4 bu = {0, 0, 0, 0};
    unsigned wc[4], xb[2], bb, actv;
    {
      const int lane = lane_id(), g = lane >> 4, q = (lane & 15) >> 2, pp = lane & 3, f = (q >> 1) | ((g & 1) << 1);
      for (int c = 0; c < 4; ++c) wc[c] = L_W + (8 * g + q) * 128 + ((c ^ f) * 32) + pp * 8;
      for (int k = 0; k < 2; ++k) xb[k] = L_A + (32 * w + (lane & 15)) * 128 + (((4 * k + g) ^ ((lane & 15) >> 1)) * 16);
      bb = KA ? L_GATE + 16 * g : L_B1 + 8 * g + (j & 1) * 64;
    }
    const unsigned acts = WS_ACT + (rt * 8 + 2 * w) * (72 * 1024);
    if (PFB) bar0();
    sfor<0, NQ + LAG>([&](auto ic) {
      constexpr int n = ic.v;
      if constexpr (n < NQ) {
        constexpr int st = n >> 4, r = n & 15;
        if constexpr (r == 0) {
          bar();
          if constexpr (st == 12) {
            const int lane = lane_id(), g = lane >> 4, q = (lane & 15) >> 2, pp = lane & 3;
            for (int c = 0; c < 2; ++c) wc[c] = L_W + (st % 5) * SLOT + (8 * g + q) * 64 + ((c ^ (g & 1)) * 32) + pp * 8;
            bb = L_B1 + 8 * g + ((j & 1) ? 0 : 384);
          } else if constexpr (st > 0) {
            for (int c = 0; c < (st < 12 ? 4 : 2); ++c) { wc[c] += (st % 5) ? SLOT : -4 * SLOT; asm volatile("" : "+v"(wc[c])); }
          }
          if constexpr (!KA && st == 0)
            for (int t = 0; t < 8; ++t) {
              acc[t][0] = acc[t][1] = unpk4(*(const LAS u32x2*)(size_t)(bb + t * 32));
              asm volatile("" : "+v"(acc[t][0]), "+v"(acc[t][1]));
            }
          if constexpr (st == 12)
            for (int t = 0; t < 2; ++t) { f32x4 b = unpk4(*(const LAS u32x2*)(size_t)(bb + t * 32)); acc3[t][0] = b; acc3[t][1] = b; asm volatile("" : "+v"(acc3[t][0]), "+v"(acc3[t][1])); }
        }
      }
      sfor<0, 2>([&](auto hc) {
        constexpr int mi = hc.v, m = n - LAG;
        if constexpr (m >= 0) {
          constexpr int st = m >> 4, r = m & 15;
          const bf16x8 wv = cat(wf[m % NBUF][0], wf[m % NBUF][1]);
          if constexpr (st < 8) {
            constexpr int t = r & 7, ks = 2 * st + (r >> 3);
            if constexpr (KA && ks == 0 && t < 4) mac1(acc[t][mi], xf[ks][mi], wv);
            else if constexpr (KA && ks == 0) mac0(acc[t][mi], xf[ks][mi], wv);
            else if constexpr (KA) mac(acc[t][mi], xf[ks][mi], wv);
            else mac(acc[t][mi], wv, xf[ks][mi]);
            if constexpr (KA && t == 2) { if constexpr (ks == 0) mac0(accg[mi], gf[0], xf[0][mi]); else mac(accg[mi], gf[ks & 1], xf[ks][mi]); }
          } else if constexpr (st == 8) {
            static_assert(LAG >= 4 && LAG <= 5, "bias register hand-over");
            constexpr int ks = r & 3, t = r >> 2;
            if constexpr (ks) mac(acc2[t][mi], wv, xf[ks][mi]);
            else if constexpr (KA) mac1(acc2[t][mi], wv, xf[ks][mi]);
            else macc(acc2[t][mi], wv, xf[ks][mi], bu);
          } else if constexpr (st < 12) {
            mac(acc2[r & 3][mi], wv, xf[4 * (st - 8) + (r >> 2)][mi]);
          } else {
            mac(acc3[r & 1][mi], wv, xf[8 * (st - 12) + (r >> 1)][mi]);
          }
          sb();
        }
        if constexpr (n < NQ) {
          constexpr int st = n >> 4, r = n & 15;
          if constexpr (st < 8) {
            constexpr int kk = r >> 3, t = r & 7;
            if constexpr (t == 0) xf[2 * st + kk][mi] = rd_frag(xb[kk] + (st & 3) * SLOT + mi * 2048);
            if constexpr (KA && t == 1 && mi == 1) gf[kk] = rd_frag(bb + (2 * st + kk) * 64);
            wf[n % NBUF][mi] = rd_h(wc[t & 3] + (t >> 2) * 8192 + kk * 4096 + mi * 512);
          } else if constexpr (st == 8) {
            if constexpr (!KA && (r & 3) == 0 && mi == 0) bt = *(const LAS u32x2*)(size_t)(bb + 256 + (r >> 2) * 32);
            if constexpr (!KA && (r & 3) == 3 && mi == 0) bu = unpk4(bt);
            wf[n % NBUF][mi] = rd_h(wc[r >> 2] + (r & 3) * 4096 + mi * 512);
          } else if constexpr (st < 12) {
            wf[n % NBUF][mi] = rd_h(wc[r & 3] + (r >> 2) * 4096 + mi * 512);
          } else {
            wf[n % NBUF][mi] = rd_h(wc[r & 1] + (r >> 1) * 2048 + mi * 256);
          }
        }
        if constexpr (m == 122 && mi == 0) actv = lane_id() * 16;
        if constexpr (!KA) {
          if constexpr (m == MH1 - 4 && mi == 0) tg = *(const LAS u32x2*)(size_t)L_TAG;
          if constexpr (m == MH1 && mi == 0) { vmwait<0>(); set_flag(rws, actv, F_H + fl, tg); }
          constexpr bool e1 = m >= 124 && m < 124 + 32 / EQ, e2 = m >= 192 && m < 208;
          if constexpr (e1 || e2) {
            constexpr bool whole = e1 && EQ == 2;
            constexpr int Q = e2 ? m - 192 : whole ? 2 * (m - 124) + mi : m - 124;
            constexpr int pr = Q >> 3, ei = (Q >> 2) & 1, qq = Q & 3;
            if constexpr (whole || mi == 0) {
              const f32x4 a = e1 ? acc[2 * pr + (qq >> 1)][ei] : acc2[(2 * pr + (qq >> 1)) & 3][ei];
              t0 = f32x2{ssq(a[2 * (qq & 1)]), ssq(a[2 * (qq & 1) + 1])};
            }
            if constexpr (whole || mi == 1) {
              hq[qq] = relu_pk(pk(t0[0], t0[1]));
              if constexpr (qq == 3) st_sc1(hq, rws, actv, acts + (ei * 72 + (e1 ? 4 * j : 32 + 2 * j) + pr) * 1024);
            }
          }
        } else {
          if constexpr (m >= 127 && m < 131) {
            const f32x4 a = acc[m - 123][mi];
            pvf[m - 127][2 * mi] = pk(a[0], a[1]); pvf[m - 127][2 * mi + 1] = pk(a[2], a[3]);
          }
          if constexpr (m == 126) asm volatile("" :: "v"(accg[mi]));
          constexpr int S = 2 * m + mi - 248;
          if constexpr (S >= 0 && S < 48) {
            constexpr int U = S / 3, k = S % 3, h = U >> 2;
            const f32x4 a4 = acc[h][(U >> 1) & 1];
            const f32x2 a = {a4[2 * (U & 1)], a4[2 * (U & 1) + 1]};
            if constexpr (k == 0) { t0 = f32x2{fma1(a[0], LOG2E), fma1(a[1], LOG2E)}; t0[0] = exp2c(t0[0]); }
            if constexpr (k == 1) { t1[0] = maxadd(a[0], t0[0], 1e-6f); sb(); t0[1] = exp2c(t0[1]); }
            if constexpr (k == 2) { t1[1] = maxadd(a[1], t0[1], 1e-6f); pkf[h][U & 3] = pk(t1[0], t1[1]); }
          }
          if constexpr (S >= 12 && S <= 48 && S % 12 == 0) {
            constexpr int h = S / 12 - 1;
            const bf16x8 kf = __builtin_bit_cast(bf16x8, pkf[h]);
            sb();
            mac0(kv, kf, __builtin_bit_cast(bf16x8, pvf[h]));
            mac0(zz, kf, one);
          }
          if constexpr (S >= 18 && S <= 54 && S % 12 == 6) {
            constexpr int h = S / 12 - 1;
            *(LAS f32x4*)(size_t)(L_A + (h * 4 + w) * 1024 + actv) = kv;
            *(LAS f32x4*)(size_t)(bb + (L_Z - L_GATE) + (h * 4 + w) * 64) = zz;
          }
          if constexpr (S == 8) { one = __builtin_bit_cast(bf16x8, u32x4{0x3f803f80u, 0x3f803f80u, 0x3f803f80u, 0x3f803f80u}); asm volatile("" : "+v"(one)); }
        }
        sb();
      });
    });
    settle();
    if constexpr (KA) {
      bar();
      for (int mi = 0; mi < 2; ++mi) {
        gq[mi] = __builtin_amdgcn_rcpf(1.f + __expf(-accg[mi][0]));
        for (int hp = 0; hp < 2; ++hp) pq[mi][hp] = pk8(phi(acc2[2 * hp][mi]), phi(acc2[2 * hp + 1][mi]));
      }
    } else {
      for (int mi = 0; mi < 2; ++mi)
        st_sc1(pk8(relu2(acc3[0][mi]), relu2(acc3[1][mi])), rws, actv, acts + (mi * 72 + 48 + j) * 1024);
    }
  }
  const int lane = lane_id(), g = lane >> 4, q = (lane & 15) >> 2, pp = lane & 3, f = (q >> 1) | ((g & 1) << 1);
  const int wm = w & 1, wn = w >> 1;
  f32x4 o[2][2];
  for (int t = 0; t < 2; ++t) { f32x4 b = unpk4(*(const LAS u32x2*)(size_t)(L_B2 + nt * 128 + wn * 64 + t * 32 + g * 8)); o[t][0] = b; o[t][1] = b; asm volatile("" : "+v"(o[t][0]), "+v"(o[t][1])); }
  const u32x2 tag = *(const LAS u32x2*)(size_t)L_TAG;
  unsigned wa[2], aa = L_A + wm * 8192 + lane * 16, wslot = KA ? 2 : 4;
  for (int c = 0; c < 2; ++c) wa[c] = L_W + wslot * SLOT + (4 * g + q) * 128 + (((2 * wn + c) ^ f) * 32) + pp * 8;
  bf16x8 af[2][4][2];
  s16x4 bf[2][4][2][2];
  auto stage2 = [&](auto sc, auto rdc, auto mmc) {
    constexpr int s = sc.v;
    if constexpr (rdc.v) bar();
    sfor<0, 16>([&](auto kc) {
      constexpr int u = kc.v >> 2, c = (kc.v >> 1) & 1, mi = kc.v & 1;
      if constexpr (mmc.v) { mac(o[c][mi], cat(bf[1 - s][u][c][0], bf[1 - s][u][c][1]), af[1 - s][u][mi]); sb(); }
      if constexpr (rdc.v && kc.v < 12) {
        sfor<2 * kc.v, 2 * kc.v + 2>([&](auto rc) {
          constexpr int ru = rc.v / 6, k = rc.v % 6;
          if constexpr (k < 2) af[s][ru][k] = rd_frag(aa + k * 4096 + ru * 1024);
          else bf[s][ru][(k - 2) >> 1][k & 1] = rd_h(wa[(k - 2) >> 1] + ru * 4096 + (k & 1) * 2048);
        });
      }
      sb();
    });
    if constexpr (rdc.v) {
      const int d = wslot == 4 ? -4 * SLOT : SLOT;
      wslot = wslot == 4 ? 0 : wslot + 1;
      wa[0] += d; wa[1] += d;
      aa = L_A + ((aa - L_A + SLOT) & (4 * SLOT - 1));
      asm volatile("" : "+v"(wa[0]), "+v"(wa[1]), "+v"(aa));
    }
  };
  static_assert((TH2A & TH2B & ~TO & 1) == 1 && TO >= 2 && TO + 2 <= 10, "hooks sit behind fixed halves of the loop");
  stage2(IC<0>{}, IC<1>{}, IC<0>{});
#pragma unroll 1
  for (int t = 1; t < 17; t += 2) {
    if (t == 15) {
      settle();
      for (int mi = 0; mi < 2; ++mi) {
        const float s = 1.f - *(const LAS float*)(size_t)(L_G64 + (wm * 32 + mi * 16 + (lane & 15)) * 4);
        o[0][mi] *= s; o[1][mi] *= s;
      }
    }
    stage2(IC<1>{}, IC<1>{}, IC<1>{});
    if (!KA && t == TH2A) { vmwait<2>(); set_flag(rws, lane * 16, F_H + 512 + fl, tag); }
    if (!KA && t == TH2B) { vmwait<1>(); set_flag(rws, lane * 16, F_H + 1024 + fl, tag); }
    stage2(IC<0>{}, IC<1>{}, IC<1>{});
    if (KA && t + 1 == TO + 2) { vmwait<0>(); set_flag(rws, lane * 16, F_O + fl, tag); }
    if (KA && t + 1 == TO) {
      const unsigned acts = WS_ACT + (rt * 8 + 2 * w) * (72 * 1024);
      for (int hp = 0; hp < 2; ++hp) {
        const u32x4 k0 = *(const LAS u32x4*)(size_t)(L_KVB + (2 * hp) * 1024 + lane * 16);
        const u32x4 k1 = *(const LAS u32x4*)(size_t)(L_KVB + (2 * hp + 1) * 1024 + lane * 16);
        const bf16x8 kv0 = __builtin_bit_cast(bf16x8, u32x4{k0[0], k0[1], 0, 0}), z0 = __builtin_bit_cast(bf16x8, u32x4{k0[2], k0[3], 0, 0});
        const bf16x8 kv1 = __builtin_bit_cast(bf16x8, u32x4{0, 0, k1[0], k1[1]}), z1 = __builtin_bit_cast(bf16x8, u32x4{0, 0, k1[2], k1[3]});
        for (int mi = 0; mi < 2; ++mi) {
          const bf16x8 pf = __builtin_bit_cast(bf16x8, pq[mi][hp]);
          const f32x4 zero = {0, 0, 0, 0};
          f32x4 n0 = mfma(kv0, pf, zero), d0 = mfma(z0, pf, zero), n1 = mfma(kv1, pf, zero), d1 = mfma(z1, pf, zero);
          n0 *= gq[mi] * __builtin_amdgcn_rcpf(d0[0] + 1e-6f);
          n1 *= gq[mi] * __builtin_amdgcn_rcpf(d1[0] + 1e-6f);
          st_sc1(pk8(n0, n1), rws, lane * 16, acts + (mi * 72 + 56 + 2 * j + hp) * 1024);
        }
      }
      __builtin_amdgcn_raw_buffer_store_b32(__float_as_uint(lane < 16 ? gq[0] : gq[1]), rws, (j == 0 && lane < 32) ? lane * 4 : WS_END,
                                            WS_G + (rt * 128 + 32 * w) * 4, 16);
    }
  }
  stage2(IC<1>{}, IC<1>{}, IC<1>{});
  stage2(IC<0>{}, IC<0>{}, IC<1>{});
  settle();
  const unsigned ob = L_OUT + w * 2048;
  for (int mi = 0; mi < 2; ++mi)
    for (int c = 0; c < 2; ++c) {
      const int row = mi * 16 + (lane & 15);
      *(LAS u32x2*)(size_t)(ob + row * 64 + (((2 * c + (g >> 1)) ^ ((row >> 1) & 3)) * 16) + (g & 1) * 8) =
          u32x2{pk(o[c][mi][0], o[c][mi][1]), pk(o[c][mi][2], o[c][mi][3])};
    }
  for (int h = 0; h < 2; ++h) {
    const int row = h * 16 + (lane >> 2);
    const u32x4 v = *(const LAS u32x4*)(size_t)(ob + row * 64 + (((lane & 3) ^ ((row >> 1) & 3)) * 16));
    *(u32x4*)((char*)p_out + (size_t)(mt * 64 + wm * 32 + row) * 1024 + nt * 128 + wn * 64 + (lane & 3) * 16) = v;
  }
  DUMP();
}

__global__ void __launch_bounds__(512, 2)
fused_kernel(const char* p_x, const char* p_wqkv, const char* p_wo, const char* p_w1, const char* p_b1,
             const char* p_w2, const char* p_b2, const char* p_gate, __hip_bfloat16* p_out, char* p_ws) {
  __shared__ __attribute__((aligned(16))) char smem[L_TOTAL];
  asm volatile("" :: "v"((LAS char*)smem) : "memory");
  const int lane = threadIdx.x & 63, w = __builtin_amdgcn_readfirstlane(threadIdx.x >> 6);
  if (w == 7) {
    const char* code;
    asm volatile("s_getpc_b64 %0" : "=s"(code));
    touch(((blockIdx.x >> 3 & 7) * 64 + lane) * 128, mkrs(code, CODE_BYTES), 0);
  }
  sb();
  const int b = blockIdx.x, xcd = b & 7, bi = b >> 3;
  const int rt = (xcd & 1) * 8 + (bi & 7), j = (xcd >> 1) * 2 + (bi >> 4);
  const bool ka = ((bi >> 3) & 1) == 0;
  const int mt = xcd * 4 + (bi & 3), nt = bi >> 2;
  if (w < 4) {
    if (ka) compute<true>(DGA p_ws, p_out, w, rt, j, mt, nt);
    else compute<false>(DGA p_ws, p_out, w, rt, j, mt, nt);
  } else if (w == 4) {
    const unsigned pitch = ka ? 3072 : 3584;
    rsrc_t rs = mkrs(ka ? p_wqkv : p_w1, 512 * pitch);
    const unsigned r8 = lane >> 3, c32 = (lane & 7) >> 1, c16 = (lane & 1) * 16;
    unsigned v[8], slot = L_W;
    for (int p = 0; p < 8; ++p) v[p] = (r8 + 8 * p) * pitch + (((c32 ^ ((r8 >> 1) & 1)) * 32) ^ ((p & 1) * 64)) + c16 - (p & 3) * 1024;
    const unsigned cb = j * 448 + (j & 1) * 64;
    unsigned sa = ka ? 1024 + j * 128 : cb, sd = ka ? 1024 : 128, step = 64 * pitch;
    auto issue = [&](bool nt_ = NTW) {
      if (nt_) dma16_nt(slot, v, rs, sa, sa + sd); else dma16(slot, v, rs, sa, sa + sd);
      slot = slot == L_W + 4 * SLOT ? L_W : slot + SLOT;
      sa += step;
    };
    __builtin_amdgcn_s_setprio(PRW);
#pragma unroll 1
    for (int s = 0; s < 8; ++s) {
      if (PFB && s == AH) bar0();
      if (s >= AH) { PRE(); vmwait<16 * (AH - 1)>(); bar(); }
      issue();
    }
    sa = ka ? j * 128 : cb + 256; sd = 64 * pitch; step = 128 * pitch;
    if (AH == 2) {
      vmwait<16>(); bar(); issue(); issue();
      vmwait<32>(); bar(); issue(); issue();
    } else {
      vmwait<32>(); bar(); issue(); issue();
      vmwait<48>(); bar(); issue();
      vmwait<48>(); bar(); issue();
    }
    if ((PF & 4) && ka) {
      const int a = (b & 63) | (b >> 7) << 6;
      touch(lane * 128, mkrs(p_w2 + a * 14336, 14336), 0);
      touch(lane * 128 + 8192, mkrs(p_w2 + a * 14336, 14336), 0);
      touch(lane * 128, mkrs(p_wo + a * 4096, 4096), 0);
    }
    if (!ka) {
      for (int p = 0; p < 8; ++p) v[p] = ((lane >> 2) + 16 * p) * 3584 + ((((lane & 3) >> 1) ^ (lane >> 5)) * 32) + c16 - (p & 3) * 1024;
      sa = j * 448 + ((j & 1) ? 0 : 384); sd = 128 * 3584; step = 256 * 3584;
#pragma unroll 1
      for (int s = 0; s < 2; ++s) { PRE(); vmwait<48>(); bar(); issue(); }
    }
    for (int p = 0; p < 8; ++p) v[p] = (r8 + 8 * p) * 1024 + ((c32 ^ (r8 >> 1)) * 32) + c16 + nt * 128 - (p & 3) * 1024;
    rs = mkrs(p_w2, 1792 * 1024);
    sa = 0; sd = 64 * 1024; step = 256 * 1024;
    unsigned alt = 448 * 1024;
#pragma unroll 1
    for (int seg = 0; seg < 5; ++seg) {
      int n = 5;
      if (seg == 1) { n = 3; if (ka) bar(); }
      if (seg == 2) { n = 4; sa = 128 * 1024; sd = 256 * 1024; step = 448 * 1024; alt = 2 * step; }
      if (seg == 3) { n = 2; sa = 192 * 1024; sd = 448 * 1024; step = 896 * 1024; alt = 2 * step; }
      if (seg == 4) { n = 4; sa = 0; sd = 64 * 1024; step = 128 * 1024; alt = 2 * step; rs = mkrs(p_wo, 512 * 1024); }
#pragma unroll 1
      do {
        PRE(); vmwait<48>(); bar();
        issue(false);
        step = alt - step;
      } while (--n);
    }
    vmwait<48>(); bar();
    vmwait<32>(); bar();
    vmwait<16>(); bar();
    vmwait<0>(); bar();
    DUMP();
  } else if (w == 5) {
    unsigned v[8];
    for (int p = 0; p < 8; ++p) v[p] = ((lane >> 3) + 8 * p) * 1024 + ((((lane & 7) ^ (lane >> 4)) * 16) ^ ((p & 1) * 64)) - (p & 3) * 1024;
    const rsrc_t rx = mkrs(p_x, 2048 * 1024), rws = mkrs(p_ws, WS_END);
#pragma unroll 1
    for (int s = 0; s < 8; ++s) {
      if (PFB && s == AH) bar0();
      if (s >= AH) { PRE(); vmwait<16 * (AH - 1)>(); bar(); }
      dma16(L_A + (s & 3) * SLOT, v, rx, rt * 131072 + s * 128, rt * 131072 + s * 128 + 65536);
    }
    if (AH == 3) { vmwait<32>(); bar(); }
    vmwait<16>(); bar();
    vmwait<0>(); bar();
    for (int p = 0; p < 8; ++p) v[p] = lane * 16 + (p >> 2) * (72 * 1024);
#pragma unroll 1
    for (int i = ka ? CH1A : CH1B; i >= 8; --i) bar();
    static_assert(CH1A + 1 == PA && CH1B + 1 == PB, "no barrier between the poll and phase 2");
    unsigned sa = WS_ACT + mt * 4 * 72 * 1024;
#pragma unroll 1
    for (int t = 0; t < 4; ++t, sa += 4096) {
      if (t == 2) { PRE(); vmwait<16>(); bar(); }
      dma16_sc1(L_A + t * SLOT, v, rws, sa, sa + 144 * 1024);
    }
#pragma unroll 1
    for (int t = 4; t < 18; ++t, sa += 4096) {
      const unsigned l = L_A + (t & 3) * SLOT, s2 = sa + 144 * 1024;
      PRE(); vmwait<32>(); bar();
      dma16_sc1(l, v, rws, sa, s2);
    }
    vmwait<32>(); bar();
    vmwait<16>(); bar();
    vmwait<0>(); bar();
    DUMP();
  } else {
    const rsrc_t rws = mkrs(p_ws, WS_END);
    const int hp = w - 6;
    if (hp == 0) {
      dma(ka ? L_GATE : L_B2, lane * 16, mkrs(ka ? p_gate : p_b2, 1024), 0);
      dma(ka ? L_B2 : L_B1, ka ? lane * 16 : lane < 28 ? lane * 16 + j * 448 : 4096, mkrs(ka ? p_b2 : p_b1, ka ? 1024 : 3584), 0);
      if (!ka) { if (PFB) bar0(); vmwait<0>(); return; }
    }
    const u64 tag64 = __hip_atomic_load((const GAS u64*)p_ws, __ATOMIC_RELAXED, __HIP_MEMORY_SCOPE_AGENT) + 1;
    const u32x2 tag = __builtin_bit_cast(u32x2, tag64), none = ~tag;
    if (PFB) bar0();
    if (hp) {
      if (PF & 1) touch(lane * 128, mkrs(p_x, 2048 * 1024), (b & 1) * 1048576 + (b >> 1) * 8192);
      if (PF & 1) touch(lane * 128, mkrs(p_wqkv + b * 6144, 6144), 0);
      if (PF & 1) touch(lane * 128, mkrs(p_w1 + b * 7168, 7168), 0);
      if (PF & 2) touch(lane * 128, mkrs(p_w2 + b * 7168, 7168), 0);
      if (PF & 2) touch(lane * 128, mkrs(p_wo + b * 2048, 2048), 0);
    } else {
      vmwait<0>();
    }
    const unsigned fo = (lane & 15) * 64;
    auto bars = [&](int n) {
#pragma unroll 1
      for (; n > 0; --n) bar();
    };
    bar();
    bars(6);
    *(LAS u32x2*)(size_t)L_TAG = tag;
    if (ka) {
      bars(KV_LDS - 6);
      {
        f32x4 p[3][4];
        for (int v = 0; v < 4; ++v) {
          for (int hh = 0; hh < 2; ++hh) p[hh][v] = *(const LAS f32x4*)(size_t)(L_A + ((2 * hp + hh) * 4 + v) * 1024 + lane * 16);
          p[2][v] = *(const LAS f32x4*)(size_t)(L_Z + (hp * 8 + (lane & 4) + v) * 64 + (lane & 3) * 16);
        }
        sb();
        for (int k = 0; k < 3; ++k) p[k][0] = (p[k][0] + p[k][1]) + (p[k][2] + p[k][3]);
        st_sc1(pk8(p[0][0], p[1][0]), rws, lane * 16, WS_KV + ((j * 2 + hp) * 16 + rt) * 1024);
        st_sc1(__builtin_bit_cast(u32x4, p[2][0]), rws, lane < 8 ? (lane & 4) * 256 + (lane & 3) * 16 : WS_END, WS_Z + (j * 2 + hp) * 2048 + rt * 64);
      }
      bars(CH1A - KV_LDS - 1);
      vmwait<0>();
      set_flag(rws, lane * 16, F_KV + (j * 2 + hp) * 16 + rt, tag);
      if (hp) poll(rws, fo, F_H + mt * 16, tag, none);
      bars(KV_POLL - CH1A);
      const u32x2 early = ld_flag(rws, fo, F_KV + (j * 2 + hp) * 16);
      bar();
      poll(rws, fo, F_KV + (j * 2 + hp) * 16, tag, early);
      u32x4 gv[16], gz[2];
      for (int hh = 0; hh < 2; ++hh) gz[hh] = ld_sc1(rws, lane * 16 + hh * 1024, WS_Z + (j * 2 + hp) * 2048);
      for (int v = 0; v < 16; ++v) gv[v] = ld_sc1(rws, lane * 16 + (v & 3) * 1024, WS_KV + ((j * 2 + hp) * 16 + (v & 12)) * 1024);
      const u32x2 e2 = ld_flag(rws, fo, F_H + 512 + mt * 16);
      bar();
      bar();
      for (int hh = 0; hh < 2; ++hh) {
        const unsigned e = (lane >> 4) == ((lane & 15) >> 2) ? ((lane & 1) ? 0x3f800000u : 0x3f80u) : 0u, e0 = (lane & 2) ? 0u : e, e1 = (lane & 2) ? e : 0u;
        const bf16x8 sel = __builtin_bit_cast(bf16x8, hh ? u32x4{0, 0, e0, e1} : u32x4{e0, e1, 0, 0});
        f32x4 s = {0, 0, 0, 0}, z = __builtin_bit_cast(f32x4, gz[hh]);
        for (int v = 0; v < 16; ++v) s = mfma(sel, __builtin_bit_cast(bf16x8, gv[v]), s);
        for (int e = 0; e < 4; ++e) {
          float t = z[e];
          t += __int_as_float(__builtin_amdgcn_update_dpp(0, __float_as_int(t), 0x124, 0xf, 0xf, false));
          t += __int_as_float(__builtin_amdgcn_update_dpp(0, __float_as_int(t), 0x128, 0xf, 0xf, false));
          float u[4];
          for (int r = 0; r < 4; ++r) u[r] = __int_as_float(__builtin_amdgcn_ds_bpermute((r * 16 + (lane >> 4)) * 4, __float_as_int(t)));
          z[e] = (u[0] + u[1]) + (u[2] + u[3]);
        }
        *(LAS u32x4*)(size_t)(L_KVB + (2 * hp + hh) * 1024 + lane * 16) = pk8(s, z);
      }
      static_assert(KV_RDY <= PA + 5, "wave 7 has to be back for the second h poll");
      if (hp == 0) { bar(); DUMP(); return; }
      u32x2 e3 = e2;
      if (KV_RDY < PA + 5) { e3 = ld_flag(rws, fo, F_H + 512 + mt * 16); bars(PA + 5 - KV_RDY); }
      poll(rws, fo, F_H + 512 + mt * 16, tag, e3);
    } else {
      bars(CH1B - 7);
      poll(rws, fo, F_H + mt * 16, tag, none);
      bars(PB + 3 - CH1B);
      const u32x2 e2 = ld_flag(rws, fo, F_H + 512 + mt * 16);
      bars(2);
      poll(rws, fo, F_H + 512 + mt * 16, tag, e2);
    }
    bars(2);
    u32x2 e = ld_flag(rws, fo, F_H + 1024 + mt * 16);
    bars(2);
    poll(rws, fo, F_H + 1024 + mt * 16, tag, e);
    e = ld_flag(rws, fo, F_O + mt * 16);
    bars(2);
    poll(rws, fo, F_O + mt * 16, tag, e);
    const unsigned gval = __builtin_amdgcn_raw_buffer_load_b32(rws, lane * 4, WS_G + mt * 256, 16);
    u32x2 all[2];
    if (b == 0) for (int k = 0; k < 2; ++k) all[k] = ld_flag(rws, (lane >> 3) * 2048 + (lane & 7) * 128, F_H + k * 256);
    bars(2);
    *(LAS unsigned*)(size_t)(L_G64 + lane * 4) = gval;
    bar();
    if (b == 0) {
      for (int k = 0; k < 2; ++k) poll(rws, (lane >> 3) * 2048 + (lane & 7) * 128, F_H + k * 256, tag, all[k]);
      __builtin_amdgcn_raw_buffer_store_b64(tag, rws, lane << 24, 0, 16);
    }
    DUMP();
  }
}

extern "C" void kernel_launch(void* const* d_in, const int* in_sizes, int n_in,
                              void* d_out, int out_size, void* d_ws, size_t ws_size,
                              hipStream_t stream) {
  fused_kernel<<<256, 512, 0, stream>>>((const char*)d_in[0], (const char*)d_in[1], (const char*)d_in[2],
                                        (const char*)d_in[4], (const char*)d_in[5], (const char*)d_in[6],
                                        (const char*)d_in[7], (const char*)d_in[8],
                                        (__hip_bfloat16*)d_out, (char*)d_ws + (WSTAIL ? (ws_size - (32u << 20)) & ~(size_t)0xfffff : 0));
}
```

```cpp
#include <hip/hip_runtime.h>
#include <hip/hip_bf16.h>

#define LAS __attribute__((address_space(3)))
#define GAS __attribute__((address_space(1)))
#define DEVI __device__ __forceinline__
typedef __attribute__((ext_vector_type(8))) __bf16 bf16x8;
typedef __attribute__((ext_vector_type(2))) __bf16 bf16x2;
typedef __attribute__((ext_vector_type(4))) float f32x4;
typedef __attribute__((ext_vector_type(2))) float f32x2;
typedef __attribute__((ext_vector_type(4))) short s16x4;
typedef __attribute__((ext_vector_type(4))) unsigned u32x4;
typedef __attribute__((ext_vector_type(2))) unsigned u32x2;
typedef unsigned long long u64;
typedef __amdgpu_buffer_rsrc_t rsrc_t;

constexpr int SLOT = 16384;
constexpr int L_W = 0;
constexpr int L_A = 5 * SLOT;
constexpr int L_M = 9 * SLOT;
constexpr int L_GATE = L_M, L_B1 = L_M + 1024, L_B2 = L_M + 2048, L_Z = L_M + 3072;
constexpr int L_KVB = L_M + 4096;
constexpr int L_OUT = L_M + 8192;
constexpr int L_G64 = L_GATE;
constexpr int L_TAG = L_B1 + 1008;
constexpr int L_TOTAL = 10 * SLOT;
constexpr int WS_FLAG = 512 * 1024 + 256 * 1024;
constexpr int F_H = 0;
constexpr int F_O = 1536;
constexpr int F_KV = 2048;
constexpr int WS_G = 128 * 1024;
constexpr int WS_Z = 144 * 1024;
constexpr int WS_KV = 256 * 1024;
constexpr int WS_ACT = 1024 * 1024;
constexpr int WS_END = WS_ACT + 128 * 72 * 1024;

#define AH 2
#define MH1 164
#define EQ 2
#define PF 13
#define PFB 0
#define NTW 0
#define PRW 1
#define OST 17
#define TH2A -1
#define TH2B 3
#define TOP 4
#define SLP 8
#define RS 12
#define EL 1
constexpr int PA = 12, PB = 14;
constexpr int KV_LDS = 9;
#define ORD 0
#define NTX 0
#define PS 8
#define PW 4
#define SC 3
#define T0 3
#define SLK 10
#define TO 2
constexpr int LAG = 5, NBUF = 6;
constexpr int CODE_BYTES = 40704;

template <int I> struct IC { static constexpr int v = I; };
template <int B, int E, class F> DEVI void sfor(F&& f) {
  if constexpr (B < E) { f(IC<B>{}); sfor<B + 1, E>(f); }
}
DEVI void bar0() {
  __builtin_amdgcn_sched_barrier(0);
  asm volatile("" ::: "memory");
  __builtin_amdgcn_s_waitcnt(0xc07f);
  asm volatile("s_barrier" ::: "memory");
  __builtin_amdgcn_sched_barrier(0);
}
#define bar() bar0()
template <int N> DEVI void vmwait() { asm volatile("s_waitcnt vmcnt(%0)" :: "i"(N) : "memory"); }
DEVI void sb() { __builtin_amdgcn_sched_barrier(0); }
DEVI int lane_id() { int l; asm volatile("v_mbcnt_lo_u32_b32 %0, -1, 0\n\tv_mbcnt_hi_u32_b32 %0, -1, %0" : "=v"(l)); return l; }
DEVI rsrc_t mkrs(const void* p, unsigned n) { return __builtin_amdgcn_make_buffer_rsrc((void*)p, 0, n, 0x00020000); }
DEVI void dma(unsigned lds, unsigned voff, rsrc_t rs, unsigned soff) {
  asm volatile("s_mov_b32 m0, %0\n\ts_nop 0\n\tbuffer_load_dwordx4 %1, %2, %3 offen lds" :: "s"(lds), "v"(voff), "s"(rs), "s"(soff) : "memory");
}
DEVI void touch(unsigned voff, rsrc_t rs, unsigned soff) {
  asm volatile("s_mov_b32 m0, %0\n\ts_nop 0\n\tbuffer_load_dword %1, %2, %3 offen lds" :: "s"(L_OUT), "v"(voff), "s"(rs), "s"(soff) : "memory");
}
#define DMA4(a, b, c, d, so, fl) \
  "s_nop 0\n\tbuffer_load_dwordx4 " a ", %8, " so " offen " fl "\n\tbuffer_load_dwordx4 " b ", %8, " so " offen offset:1024 " fl \
  "\n\tbuffer_load_dwordx4 " c ", %8, " so " offen offset:2048 " fl "\n\ts_waitcnt vmcnt(62)\n\tbuffer_load_dwordx4 " d ", %8, " so " offen offset:3072 " fl "\n\t"
#define DMA16(fl) \
  asm volatile("s_mov_b32 m0, %11\n\t" DMA4("%0", "%1", "%2", "%3", "%9", fl) "s_add_u32 m0, m0, 0x1000\n\t" DMA4("%4", "%5", "%6", "%7", "%9", fl) \
               "s_add_u32 m0, m0, 0x1000\n\t" DMA4("%0", "%1", "%2", "%3", "%10", fl) "s_add_u32 m0, m0, 0x1000\n\t" DMA4("%4", "%5", "%6", "%7", "%10", fl) \
               :: "v"(v[0]), "v"(v[1]), "v"(v[2]), "v"(v[3]), "v"(v[4]), "v"(v[5]), "v"(v[6]), "v"(v[7]), "s"(rs), "s"(sa), "s"(sb_), "s"(lds) : "memory", "scc")
DEVI void dma16(unsigned lds, const unsigned (&v)[8], rsrc_t rs, unsigned sa, unsigned sb_) { DMA16("lds"); }
DEVI void dma16_nt(unsigned lds, const unsigned (&v)[8], rsrc_t rs, unsigned sa, unsigned sb_) { DMA16("nt lds"); }
DEVI void dma16_sc1(unsigned lds, const unsigned (&v)[8], rsrc_t rs, unsigned sa, unsigned sb_) { DMA16("sc1 lds"); }
DEVI bf16x8 rd_frag(unsigned a) { return *(const LAS bf16x8*)(size_t)a; }
DEVI f32x4 mfma(bf16x8 a, bf16x8 b, f32x4 c) { return __builtin_amdgcn_mfma_f32_16x16x32_bf16(a, b, c, 0, 0, 0); }
DEVI f32x4 mfma16(unsigned a0, unsigned a1, unsigned b0, unsigned b1) {
  return __builtin_amdgcn_mfma_f32_16x16x16bf16_1k(__builtin_bit_cast(s16x4, u32x2{a0, a1}), __builtin_bit_cast(s16x4, u32x2{b0, b1}), f32x4{0, 0, 0, 0}, 0, 0, 0);
}
DEVI float mul1(float a, float b) { float r; asm("v_mul_f32 %0, %1, %2" : "=v"(r) : "v"(a), "v"(b)); return r; }
DEVI void mac(f32x4& c, bf16x8 a, bf16x8 b) { asm volatile("v_mfma_f32_16x16x32_bf16 %0, %1, %2, %0" : "+v"(c) : "v"(a), "v"(b)); }
DEVI void mac0(f32x4& d, bf16x8 a, bf16x8 b) { asm volatile("v_mfma_f32_16x16x32_bf16 %0, %1, %2, 0" : "=&v"(d) : "v"(a), "v"(b)); }
DEVI void mac1(f32x4& d, bf16x8 a, bf16x8 b) { asm volatile("v_mfma_f32_16x16x32_bf16 %0, %1, %2, 1.0" : "=&v"(d) : "v"(a), "v"(b)); }
DEVI void macc(f32x4& d, bf16x8 a, bf16x8 b, f32x4 c) { asm volatile("v_mfma_f32_16x16x32_bf16 %0, %1, %2, %3" : "=&v"(d) : "v"(a), "v"(b), "v"(c)); }
DEVI void settle() { asm volatile("s_nop 7\n\ts_nop 1"); sb(); }
DEVI s16x4 rd_h(unsigned a) { return __builtin_amdgcn_ds_read_tr16_b64_v4i16((LAS s16x4*)(size_t)a); }
DEVI bf16x8 cat(s16x4 lo, s16x4 hi) { return __builtin_bit_cast(bf16x8, __builtin_shufflevector(lo, hi, 0, 1, 2, 3, 4, 5, 6, 7)); }
DEVI unsigned pk(float a, float b) { f32x2 v = {a, b}; return __builtin_bit_cast(unsigned, __builtin_convertvector(v, bf16x2)); }
DEVI u32x4 pk8(f32x4 a, f32x4 b) { return u32x4{pk(a[0], a[1]), pk(a[2], a[3]), pk(b[0], b[1]), pk(b[2], b[3])}; }
DEVI f32x4 unpk4(u32x2 v) {
  return f32x4{__uint_as_float(v[0] << 16), __uint_as_float(v[0] & 0xffff0000u), __uint_as_float(v[1] << 16), __uint_as_float(v[1] & 0xffff0000u)};
}
DEVI float ssq(float a) { float r; asm("v_mul_f32_e64 %0, %1, |%1|" : "=v"(r) : "v"(a)); return r; }
DEVI unsigned relu_pk(unsigned a) { unsigned r; asm("v_pk_max_i16 %0, %1, 0" : "=v"(r) : "v"(a)); return r; }
DEVI float fma1(float a, float l) { float r; asm("v_fma_f32 %0, %1, %2, -%2" : "=v"(r) : "v"(a), "s"(l)); return r; }
DEVI float maxadd(float a, float e, float c) { float r; asm("v_max_f32 %0, %1, %2\n\tv_add_f32 %0, %3, %0" : "=v"(r) : "v"(a), "v"(e), "s"(c)); return r; }
DEVI float relu(float a) { return __int_as_float(max(__float_as_int(a), 0)); }
DEVI f32x4 relu2(f32x4 a) { f32x4 r; for (int i = 0; i < 4; ++i) r[i] = relu(a[i]); return r * r; }
constexpr float LOG2E = 1.44269504089f;
DEVI float exp2c(float a) { return __builtin_amdgcn_fmed3f(__builtin_amdgcn_exp2f(a), 0.f, 1.f); }
DEVI f32x4 phi(f32x4 a1) {
  f32x4 e = a1 * LOG2E - LOG2E;
  for (int i = 0; i < 4; ++i) e[i] = __builtin_amdgcn_fmed3f(a1[i], exp2c(e[i]), __builtin_inff());
  return e + 1e-6f;
}
template <int AUX = 16> DEVI void st_sc1(u32x4 v, rsrc_t rs, unsigned voff, unsigned soff) {
  __builtin_amdgcn_raw_buffer_store_b128(v, rs, voff, soff, AUX);
  asm volatile("s_nop 1" :: "v"(v) : "memory");
}
DEVI u32x4 ld_sc1(rsrc_t rs, unsigned voff, unsigned soff) { return __builtin_amdgcn_raw_buffer_load_b128(rs, voff, soff, 16); }
DEVI void set_flag(rsrc_t rws, unsigned l16, int idx, u32x2 tag) {
  __builtin_amdgcn_raw_buffer_store_b64(tag, rws, l16 << 20, WS_FLAG + idx * 64, 16);
}
DEVI u32x2 ld_flag(rsrc_t rws, unsigned voff, int idx) { return __builtin_amdgcn_raw_buffer_load_b64(rws, voff, WS_FLAG + idx * 64, 16); }
DEVI void poll(rsrc_t rws, unsigned voff, int idx, u32x2 tag, u32x2 v) {
#pragma unroll 1
  for (unsigned spins = 0; spins < (1u << 17); ++spins) {
    if (__all(v[0] == tag[0] && v[1] == tag[1])) break;
    asm volatile("" ::: "memory");
    v = ld_flag(rws, voff, idx);
  }
  asm volatile("" ::: "memory");
}

template <bool KA>
DEVI void compute(char* p_ws, __hip_bfloat16* p_out, int w, int rt, int j, int mt, int nt) {
  const rsrc_t rws = mkrs(p_ws, WS_END);
  const int fl = (rt * 2 + (w >> 1)) * 16 + j * 2 + (w & 1);
  u32x4 pq[2][2];
  float gq[2];
  {
    constexpr int NQ = KA ? 192 : 224;
    bf16x8 xf[16][2], gf[2], one;
    s16x4 wf[NBUF][2];
    f32x2 t0, t1;
    f32x4 kv, zz;
    u32x4 hq;
    f32x4 acc[8][2], acc2[4][2], acc3[2][2], accg[2];
    u32x4 pkf[4], pvf[4];
    u32x2 bt, tg, b0[8];
    f32x4 bu = {0, 0, 0, 0};
    unsigned wc[4], xb[2], bb, actv;
    {
      const int lane = lane_id(), g = lane >> 4, q = (lane & 15) >> 2, pp = lane & 3, f = (q >> 1) | ((g & 1) << 1);
      for (int c = 0; c < 4; ++c) wc[c] = L_W + (8 * g + q) * 128 + ((c ^ f) * 32) + pp * 8;
      for (int k = 0; k < 2; ++k) xb[k] = L_A + (32 * w + (lane & 15)) * 128 + (((4 * k + g) ^ ((lane & 15) >> 1)) * 16);
      bb = KA ? L_GATE + 16 * g : L_B1 + 8 * g + (j & 1) * 64;
    }
    const unsigned acts = WS_ACT + (rt * 8 + 2 * w) * (72 * 1024);
    auto kstep = [&](auto sc) {
      constexpr int S = sc.v;
      if constexpr (S >= 0 && S < 48) {
        constexpr int U = S / 3, k = S % 3, h = U >> 2;
        const f32x4 a4 = acc[h][(U >> 1) & 1];
        const f32x2 a = {a4[2 * (U & 1)], a4[2 * (U & 1) + 1]};
        if constexpr (k == 0) { t0 = f32x2{fma1(a[0], LOG2E), fma1(a[1], LOG2E)}; t0[0] = exp2c(t0[0]); }
        if constexpr (k == 1) { t1[0] = maxadd(a[0], t0[0], 1e-6f); sb(); t0[1] = exp2c(t0[1]); }
        if constexpr (k == 2) { t1[1] = maxadd(a[1], t0[1], 1e-6f); pkf[h][U & 3] = pk(t1[0], t1[1]); }
      }
      if constexpr (S >= 16 && S <= 52 && S % 12 == 4) {
        constexpr int h = S / 12 - 1;
        const bf16x8 kf = __builtin_bit_cast(bf16x8, pkf[h]);
        sb();
        mac0(kv, kf, __builtin_bit_cast(bf16x8, pvf[h]));
        mac0(zz, kf, one);
      }
      if constexpr (S >= 22 && S <= 58 && S % 12 == 10) {
        constexpr int h = S / 12 - 1;
        *(LAS f32x4*)(size_t)(L_A + (h * 4 + w) * 1024 + actv) = kv;
        *(LAS f32x4*)(size_t)(bb + (L_Z - L_GATE) + (h * 4 + w) * 64) = zz;
      }
      if constexpr (S == 8) { one = __builtin_bit_cast(bf16x8, u32x4{0x3f803f80u, 0x3f803f80u, 0x3f803f80u, 0x3f803f80u}); asm volatile("" : "+v"(one)); }
    };
    if (PFB) bar0();
    sfor<0, NQ + LAG>([&](auto ic) {
      constexpr int n = ic.v;
      if constexpr (n < NQ) {
        constexpr int st = n >> 4, r = n & 15;
        if constexpr (r == 0) {
          bar();
          if constexpr (st == 12) {
            const int lane = lane_id(), g = lane >> 4, q = (lane & 15) >> 2, pp = lane & 3;
            for (int c = 0; c < 2; ++c) wc[c] = L_W + (st % 5) * SLOT + (8 * g + q) * 64 + ((c ^ (g & 1)) * 32) + pp * 8;
            bb = L_B1 + 8 * g + ((j & 1) ? 0 : 384);
          } else if constexpr (st > 0) {
            for (int c = 0; c < (st < 12 ? 4 : 2); ++c) { wc[c] += (st % 5) ? SLOT : -4 * SLOT; asm volatile("" : "+v"(wc[c])); }
          }
          if constexpr (st == 12)
            for (int t = 0; t < 2; ++t) { f32x4 b = unpk4(*(const LAS u32x2*)(size_t)(bb + t * 32)); acc3[t][0] = b; acc3[t][1] = b; asm volatile("" : "+v"(acc3[t][0]), "+v"(acc3[t][1])); }
        }
      }
      sfor<0, 2>([&](auto hc) {
        constexpr int mi = hc.v, m = n - LAG;
        if constexpr (m >= 0) {
          constexpr int st = m >> 4, r = m & 15;
          const bf16x8 wv = cat(wf[m % NBUF][0], wf[m % NBUF][1]);
          if constexpr (st < 8) {
            constexpr int t = r & 7, ks = 2 * st + (r >> 3);
            if constexpr (KA && ks == 0 && t < 4) mac1(acc[t][mi], xf[ks][mi], wv);
            else if constexpr (KA && ks == 0) mac0(acc[t][mi], xf[ks][mi], wv);
            else if constexpr (KA) mac(acc[t][mi], xf[ks][mi], wv);
            else if constexpr (ks == 0 && mi == 0) macc(acc[t][0], wv, xf[0][0], acc[t][1]);
            else mac(acc[t][mi], wv, xf[ks][mi]);
            if constexpr (KA && t == 2) { if constexpr (ks == 0) mac0(accg[mi], gf[0], xf[0][mi]); else mac(accg[mi], gf[ks & 1], xf[ks][mi]); }
          } else if constexpr (st == 8) {
            static_assert(LAG >= 4 && LAG <= 5, "bias register hand-over");
            constexpr int ks = r & 3, t = r >> 2;
            if constexpr (ks) mac(acc2[t][mi], wv, xf[ks][mi]);
            else if constexpr (KA) mac1(acc2[t][mi], wv, xf[ks][mi]);
            else macc(acc2[t][mi], wv, xf[ks][mi], bu);
            if constexpr (!KA && ks == 1 && mi == 1) asm volatile("" :: "v"(bu));
          } else if constexpr (st < 12) {
            mac(acc2[r & 3][mi], wv, xf[4 * (st - 8) + (r >> 2)][mi]);
          } else {
            mac(acc3[r & 1][mi], wv, xf[8 * (st - 12) + (r >> 1)][mi]);
          }
          sb();
        }
        if constexpr (KA) { kstep(IC<2 * (2 * m + mi - 248)>{}); sb(); }
        if constexpr (n < NQ) {
          constexpr int st = n >> 4, r = n & 15;
          if constexpr (st < 8) {
            constexpr int kk = r >> 3, t = r & 7;
            if constexpr (t == 0) xf[2 * st + kk][mi] = rd_frag(xb[kk] + (st & 3) * SLOT + mi * 2048);
            if constexpr (KA && t == 1 && mi == 1) gf[kk] = rd_frag(bb + (2 * st + kk) * 64);
            wf[n % NBUF][mi] = rd_h(wc[t & 3] + (t >> 2) * 8192 + kk * 4096 + mi * 512);
            if constexpr (!KA && n < 8 && mi == 0) b0[n] = *(const LAS u32x2*)(size_t)(bb + n * 32);
            if constexpr (!KA && n >= 4 && n < 12 && mi == 0) { acc[n - 4][1] = unpk4(b0[n - 4]); asm volatile("" : "+v"(acc[n - 4][1])); }
          } else if constexpr (st == 8) {
            if constexpr (!KA && (r & 3) == 0 && mi == 0) bt = *(const LAS u32x2*)(size_t)(bb + 256 + (r >> 2) * 32);
            if constexpr (!KA && (r & 3) == 3 && mi == 0) bu = unpk4(bt);
            wf[n % NBUF][mi] = rd_h(wc[r >> 2] + (r & 3) * 4096 + mi * 512);
          } else if constexpr (st < 12) {
            wf[n % NBUF][mi] = rd_h(wc[r & 3] + (r >> 2) * 4096 + mi * 512);
          } else {
            wf[n % NBUF][mi] = rd_h(wc[r & 1] + (r >> 1) * 2048 + mi * 256);
          }
        }
        if constexpr (m == 122 && mi == 0) actv = lane_id() * 16;
        if constexpr (!KA) {
          if constexpr (m == MH1 - 4 && mi == 0) tg = *(const LAS u32x2*)(size_t)L_TAG;
          if constexpr (m == MH1 && mi == 0) { vmwait<0>(); set_flag(rws, actv, F_H + fl, tg); }
          constexpr bool e1 = m >= 124 && m < 124 + 32 / EQ, e2 = m >= 192 && m < 208;
          if constexpr (e1 || e2) {
            constexpr bool whole = e1 && EQ == 2;
            constexpr int Q = e2 ? m - 192 : whole ? 2 * (m - 124) + mi : m - 124;
            constexpr int pr = Q >> 3, ei = (Q >> 2) & 1, qq = Q & 3;
            if constexpr (whole || mi == 0) {
              const f32x4 a = e1 ? acc[2 * pr + (qq >> 1)][ei] : acc2[(2 * pr + (qq >> 1)) & 3][ei];
              t0 = f32x2{ssq(a[2 * (qq & 1)]), ssq(a[2 * (qq & 1) + 1])};
            }
            if constexpr (whole || mi == 1) {
              hq[qq] = relu_pk(pk(t0[0], t0[1]));
              if constexpr (qq == 3) st_sc1(hq, rws, actv, acts + (ei * 72 + (e1 ? 4 * j : 32 + 2 * j) + pr) * 1024);
            }
          }
        } else {
          if constexpr (m >= 127 && m < 131) {
            const f32x4 a = acc[m - 123][mi];
            pvf[m - 127][2 * mi] = pk(a[0], a[1]); pvf[m - 127][2 * mi + 1] = pk(a[2], a[3]);
          }
          if constexpr (m == 126) asm volatile("" :: "v"(accg[mi]));
          sb();
          kstep(IC<2 * (2 * m + mi - 248) + 1>{});
        }
        sb();
      });
    });
    settle();
    if constexpr (KA) {
      for (int mi = 0; mi < 2; ++mi) {
        gq[mi] = __builtin_amdgcn_rcpf(1.f + __expf(-accg[mi][0]));
        for (int hp = 0; hp < 2; ++hp) pq[mi][hp] = pk8(phi(acc2[2 * hp][mi]), phi(acc2[2 * hp + 1][mi]));
      }
    } else {
      for (int mi = 0; mi < 2; ++mi)
        st_sc1(pk8(relu2(acc3[0][mi]), relu2(acc3[1][mi])), rws, actv, acts + (mi * 72 + 48 + j) * 1024);
    }
  }
  const int lane = lane_id(), g = lane >> 4, q = (lane & 15) >> 2, pp = lane & 3, f = (q >> 1) | ((g & 1) << 1);
  const int wm = w & 1, wn = w >> 1;
  f32x4 o[2][2];
  for (int t = 0; t < 2; ++t) { f32x4 b = unpk4(*(const LAS u32x2*)(size_t)(L_B2 + nt * 128 + wn * 64 + t * 32 + g * 8)); o[t][0] = b; o[t][1] = b; asm volatile("" : "+v"(o[t][0]), "+v"(o[t][1])); }
  const u32x2 tag = *(const LAS u32x2*)(size_t)L_TAG;
  unsigned wa[2], aa = L_A + wm * 8192 + lane * 16, wslot = KA ? 2 : 4;
  for (int c = 0; c < 2; ++c) wa[c] = L_W + wslot * SLOT + (4 * g + q) * 128 + (((2 * wn + c) ^ f) * 32) + pp * 8;
  bf16x8 af[2][4][2];
  s16x4 bf[2][4][2][2];
  u32x4 k0, k1;
  f32x4 nd[2][4];
  auto stage2 = [&](auto sc, auto rdc, auto mmc, auto unc) {
    constexpr int s = sc.v, uh = unc.v;
    if constexpr (rdc.v) bar();
    sfor<0, 16>([&](auto kc) {
      constexpr int u = kc.v >> 2, c = (kc.v >> 1) & 1, mi = kc.v & 1;
      if constexpr (mmc.v) { mac(o[c][mi], cat(bf[1 - s][u][c][0], bf[1 - s][u][c][1]), af[1 - s][u][mi]); sb(); }
      if constexpr (rdc.v && kc.v < 12) {
        sfor<2 * kc.v, 2 * kc.v + 2>([&](auto rc) {
          constexpr int ru = rc.v / 6, k = rc.v % 6;
          if constexpr (k < 2) af[s][ru][k] = rd_frag(aa + k * 4096 + ru * 1024);
          else bf[s][ru][(k - 2) >> 1][k & 1] = rd_h(wa[(k - 2) >> 1] + ru * 4096 + (k & 1) * 2048);
        });
      }
      if constexpr (uh >= 0) sfor<0, 2>([&](auto mc) {
        constexpr int um = mc.v, x = kc.v + 4 - 4 * um;
        f32x4 &n0 = nd[um][0], &n1 = nd[um][1], &d0 = nd[um][2], &d1 = nd[um][3];
        const u32x4 pf = pq[um][uh];
        if constexpr (um == 0 && kc.v == 0) k0 = *(const LAS u32x4*)(size_t)(L_KVB + uh * 2048 + lane * 16);
        if constexpr (um == 0 && kc.v == 1) k1 = *(const LAS u32x4*)(size_t)(L_KVB + uh * 2048 + 1024 + lane * 16);
        if constexpr (x == 7) d0 = mfma16(k0[2], k0[3], pf[0], pf[1]);
        if constexpr (x == 8) d1 = mfma16(k1[2], k1[3], pf[2], pf[3]);
        if constexpr (x == 9) n0 = mfma16(k0[0], k0[1], pf[0], pf[1]);
        if constexpr (x == 10) n1 = mfma16(k1[0], k1[1], pf[2], pf[3]);
        if constexpr (x == 11) { d0[0] = __builtin_amdgcn_rcpf(d0[0] + 1e-6f); asm volatile("" : "+v"(d0)); }
        if constexpr (x == 12) { d1[0] = __builtin_amdgcn_rcpf(d1[0] + 1e-6f); d0[0] *= gq[um]; asm volatile("" : "+v"(d0), "+v"(d1)); }
        if constexpr (x == 13) { d1[0] *= gq[um]; for (int i = 0; i < 4; ++i) n0[i] = mul1(n0[i], d0[0]); asm volatile("" : "+v"(d1)); }
        if constexpr (x == 14) for (int i = 0; i < 4; ++i) n1[i] = mul1(n1[i], d1[0]);
        if constexpr (x == 15) st_sc1(pk8(n0, n1), rws, lane * 16, WS_ACT + ((rt * 8 + 2 * w + um) * 72 + 56 + 2 * j + uh) * 1024);
      });
      sb();
    });
    if constexpr (rdc.v) {
      const int d = wslot == 4 ? -4 * SLOT : SLOT;
      wslot = wslot == 4 ? 0 : wslot + 1;
      wa[0] += d; wa[1] += d;
      aa = L_A + ((aa - L_A + SLOT) & (4 * SLOT - 1));
      asm volatile("" : "+v"(wa[0]), "+v"(wa[1]), "+v"(aa));
    }
  };
  auto hook = [&](int t, auto oc) {
    constexpr int odd = oc.v;
    if (!KA && (TH2A & 1) == odd && t == TH2A) { vmwait<2>(); set_flag(rws, lane * 16, F_H + 512 + fl, tag); }
    if (!KA && (TH2B & 1) == odd && t == TH2B) { vmwait<1>(); set_flag(rws, lane * 16, F_H + 1024 + fl, tag); }
    if (KA && (TOP & 1) == odd && t == TOP) { vmwait<0>(); set_flag(rws, lane * 16, F_O + fl, tag); }
  };
  if (KA) __builtin_amdgcn_raw_buffer_store_b32(__float_as_uint(lane < 16 ? gq[0] : gq[1]), rws, (j == 0 && lane < 32) ? lane * 4 : WS_END,
                                                WS_G + (rt * 128 + 32 * w) * 4, 16);
  constexpr IC<-1> no;
  hook(-1, IC<1>{});
  stage2(IC<0>{}, IC<1>{}, IC<0>{}, no);
  hook(0, IC<0>{});
  static_assert(TO >= 2 && TO <= 3 && TOP >= 4 && TOP >= TO + 1, "");
  if constexpr (KA) {
    sfor<1, 5>([&](auto tc) { constexpr int t = tc.v; stage2(IC<t & 1>{}, IC<1>{}, IC<1>{}, IC<(t >= TO && t < TO + 2) ? t - TO : -1>{}); });
    hook(4, IC<0>{});
  }
#pragma unroll 1
  for (int t = KA ? 5 : 1; t < 17; t += 2) {
    if (t == 15) {
      settle();
      for (int mi = 0; mi < 2; ++mi) {
        const float s = 1.f - *(const LAS float*)(size_t)(L_G64 + (wm * 32 + mi * 16 + (lane & 15)) * 4);
        o[0][mi] *= s; o[1][mi] *= s;
      }
    }
    stage2(IC<1>{}, IC<1>{}, IC<1>{}, no);
    hook(t, IC<1>{});
    stage2(IC<0>{}, IC<1>{}, IC<1>{}, no);
    hook(t + 1, IC<0>{});
  }
  stage2(IC<1>{}, IC<1>{}, IC<1>{}, no);
  stage2(IC<0>{}, IC<0>{}, IC<1>{}, no);
  settle();
  const unsigned ob = L_OUT + w * 2048;
  for (int mi = 0; mi < 2; ++mi)
    for (int c = 0; c < 2; ++c) {
      const int row = mi * 16 + (lane & 15);
      *(LAS u32x2*)(size_t)(ob + row * 64 + (((2 * c + (g >> 1)) ^ ((row >> 1) & 3)) * 16) + (g & 1) * 8) =
          u32x2{pk(o[c][mi][0], o[c][mi][1]), pk(o[c][mi][2], o[c][mi][3])};
    }
  for (int h = 0; h < 2; ++h) {
    const int row = h * 16 + (lane >> 2);
    const u32x4 v = *(const LAS u32x4*)(size_t)(ob + row * 64 + (((lane & 3) ^ ((row >> 1) & 3)) * 16));
    st_sc1<OST>(v, mkrs(p_out, 2048 * 1024), (wm * 32 + row) * 1024 + wn * 64 + (lane & 3) * 16, mt * 65536 + nt * 128);
  }
}

__global__ void __launch_bounds__(512, 2)
fused_kernel(const char* p_x, const char* p_wqkv, const char* p_wo, const char* p_w1, const char* p_b1,
             const char* p_w2, const char* p_b2, const char* p_gate, __hip_bfloat16* p_out, char* p_ws) {
  __shared__ __attribute__((aligned(16))) char smem[L_TOTAL];
  asm volatile("" :: "v"((LAS char*)smem) : "memory");
  const int lane = threadIdx.x & 63, w = __builtin_amdgcn_readfirstlane(threadIdx.x >> 6);
  if (w == 7) {
    const char* code;
    asm volatile("s_getpc_b64 %0" : "=s"(code));
    touch(((blockIdx.x >> 3 & 7) * 64 + lane) * 128, mkrs(code, CODE_BYTES), 0);
  }
  sb();
  const int b = blockIdx.x, xcd = b & 7, bi = b >> 3;
  const int rt = (xcd & 1) * 8 + (bi & 7), j = (xcd >> 1) * 2 + (ORD ? (bi >> 3) & 1 : bi >> 4);
  const bool ka = ORD == 0 ? ((bi >> 3) & 1) == 0 : ORD == 1 ? bi < 16 : bi >= 16;
  const int mt = xcd * 4 + (bi & 3), nt = (bi >> 2) ^ NTX;
  if (w < 4) {
    if (ka) compute<true>(p_ws, p_out, w, rt, j, mt, nt);
    else compute<false>(p_ws, p_out, w, rt, j, mt, nt);
  } else if (w == 4) {
    const unsigned pitch = ka ? 3072 : 3584;
    rsrc_t rs = mkrs(ka ? p_wqkv : p_w1, 512 * pitch);
    const unsigned r8 = lane >> 3, c32 = (lane & 7) >> 1, c16 = (lane & 1) * 16;
    unsigned v[8], slot = L_W;
    for (int p = 0; p < 8; ++p) v[p] = (r8 + 8 * p) * pitch + (((c32 ^ ((r8 >> 1) & 1)) * 32) ^ ((p & 1) * 64)) + c16 - (p & 3) * 1024;
    const unsigned cb = j * 448 + (j & 1) * 64;
    unsigned sa = ka ? 1024 + j * 128 : cb, sd = ka ? 1024 : 128, step = 64 * pitch;
    auto issue = [&](bool nt_ = NTW) {
      if (nt_) dma16_nt(slot, v, rs, sa, sa + sd); else dma16(slot, v, rs, sa, sa + sd);
      slot = slot == L_W + 4 * SLOT ? L_W : slot + SLOT;
      sa += step;
    };
    __builtin_amdgcn_s_setprio(PRW);
#pragma unroll 1
    for (int s = 0; s < AH; ++s) issue();
    if (PFB) bar0();
    unsigned v1[8], v2[8];
    for (int p = 0; p < 8; ++p) {
      v1[p] = ((lane >> 2) + 16 * p) * 3584 + ((((lane & 3) >> 1) ^ (lane >> 5)) * 32) + c16 - (p & 3) * 1024;
      v2[p] = (r8 + 8 * p) * 1024 + ((c32 ^ (r8 >> 1)) * 32) + c16 + nt * 128 - (p & 3) * 1024;
      asm volatile("" : "+v"(v1[p]), "+v"(v2[p]));
    }
#pragma unroll 1
    for (int s = AH; s < 8; ++s) { vmwait<16 * (AH - 1)>(); bar(); issue(); }
    sa = ka ? j * 128 : cb + 256; sd = 64 * pitch; step = 128 * pitch;
    if (AH == 2) {
      vmwait<16>(); bar(); issue(); issue();
      vmwait<32>(); bar(); issue(); issue();
    } else {
      vmwait<32>(); bar(); issue(); issue();
      vmwait<48>(); bar(); issue();
      vmwait<48>(); bar(); issue();
    }
    auto next = [&](const unsigned (&u)[8], rsrc_t r, unsigned a, unsigned d) {
      vmwait<48>(); bar();
      dma16(slot, u, r, a, a + d);
      slot = slot == L_W + 4 * SLOT ? L_W : slot + SLOT;
    };
    if (!ka) for (int s = 0; s < 2; ++s) next(v1, rs, j * 448 + ((j & 1) ? 0 : 384) + s * 256 * 3584, 128 * 3584);
    const rsrc_t r2 = mkrs(p_w2, 1792 * 1024), ro = mkrs(p_wo, 512 * 1024);
    sfor<0, 18>([&](auto kc) {
      constexpr int k = kc.v;
      constexpr int a = k < 8 ? (k >> 1) * 448 + (k & 1) * 256 : k < 12 ? 128 + (k - 8) * 448 : k < 14 ? 192 + (k - 12) * 896 : (k - 14) * 128;
      next(v2, k < 14 ? r2 : ro, a * 1024, (k < 8 ? 64 : k < 12 ? 256 : k < 14 ? 448 : 64) * 1024);
    });
    vmwait<48>(); bar();
    vmwait<32>(); bar();
    vmwait<16>(); bar();
    vmwait<0>(); bar();
  } else if (w == 5) {
    unsigned v[8];
    for (int p = 0; p < 8; ++p) v[p] = ((lane >> 3) + 8 * p) * 1024 + ((((lane & 7) ^ (lane >> 4)) * 16) ^ ((p & 1) * 64)) - (p & 3) * 1024;
    const rsrc_t rx = mkrs(p_x, 2048 * 1024), rws = mkrs(p_ws, WS_END);
#pragma unroll 1
    for (int s = 0; s < 8; ++s) {
      if (PFB && s == AH) bar0();
      if (s >= AH) { vmwait<16 * (AH - 1)>(); bar(); }
      dma16(L_A + (s & 3) * SLOT, v, rx, rt * 131072 + s * 128, rt * 131072 + s * 128 + 65536);
    }
    if (AH == 3) { vmwait<32>(); bar(); }
    vmwait<16>(); bar();
    vmwait<0>(); bar();
    for (int p = 0; p < 8; ++p) v[p] = lane * 16 + (p >> 2) * (72 * 1024);
#pragma unroll 1
    for (int i = ka ? PA - 2 : PB - 2; i >= 8; --i) bar();
    {
      __builtin_amdgcn_s_sleep(SLP);
      const u32x2 tag = *(const LAS u32x2*)(size_t)L_TAG, early = ld_flag(rws, (lane & 15) * 64, F_H + mt * 16);
      bar();
      poll(rws, (lane & 15) * 64, F_H + mt * 16, tag, early);
    }
    unsigned sa = WS_ACT + mt * 4 * 72 * 1024;
#pragma unroll 1
    for (int t = 0; t < 4; ++t, sa += 4096) {
      if (t == T0) { vmwait<16 * (T0 - 1)>(); bar(); }
      dma16_sc1(L_A + t * SLOT, v, rws, sa, sa + 144 * 1024);
    }
#pragma unroll 1
    for (int t = 4; t < 18; ++t, sa += 4096) {
      const unsigned l = L_A + (t & 3) * SLOT, s2 = sa + 144 * 1024;
      vmwait<32>(); bar();
      dma16_sc1(l, v, rws, sa, s2);
    }
    vmwait<32>(); bar();
    vmwait<16>(); bar();
    vmwait<0>(); bar();
  } else {
    const rsrc_t rws = mkrs(p_ws, WS_END);
    const int hp = w - 6;
    const unsigned fo = (lane & 15) * 64;
    auto bars = [&](int n) {
#pragma unroll 1
      for (; n > 0; --n) bar();
    };
    if (hp == 0) {
      dma(ka ? L_GATE : L_B2, lane * 16, mkrs(ka ? p_gate : p_b2, 1024), 0);
      dma(ka ? L_B2 : L_B1, ka ? lane * 16 : lane < 28 ? lane * 16 + j * 448 : 4096, mkrs(ka ? p_b2 : p_b1, ka ? 1024 : 3584), 0);
      u64 epoch = __hip_atomic_load((const GAS u64*)p_ws, __ATOMIC_RELAXED, __HIP_MEMORY_SCOPE_AGENT);
      if (PFB) bar0();
      vmwait<1>();
      bars(PW);
      asm volatile("" : "+v"(epoch));
      *(LAS u64*)(size_t)L_TAG = epoch + 1;
      bar();
      if (!ka) return;
      if (PF & 4) {
        const int a = ORD ? b & 127 : (b & 63) | (b >> 7) << 6;
        touch(lane * 128, mkrs(p_w2 + a * 14336, 14336), 0);
        touch(lane * 128 + 8192, mkrs(p_w2 + a * 14336, 14336), 0);
        touch(lane * 128, mkrs(p_wo + a * 4096, 4096), 0);
      }
      bars(KV_LDS - 1 - PW);
    } else {
      if (PFB) bar0();
      touch(lane * 128, mkrs(p_x, 2048 * 1024), (b & 1) * 1048576 + (b >> 1) * 8192);
      touch(lane * 128, mkrs(p_wqkv + b * 6144, 6144), 0);
      touch(lane * 128, mkrs(p_w1 + b * 7168, 7168), 0);
      bars(PS + 1);
      if (PF & 8) touch(lane * 1024 + ((SC - ((unsigned)(size_t)p_w2 >> 7)) & 7) * 128, mkrs(p_w2 + bi * 57344, 57344), 0);
      if (PF & 8) touch(lane * 1024 + ((SC - ((unsigned)(size_t)p_wo >> 7)) & 7) * 128, mkrs(p_wo + bi * 16384, 16384), 0);
      bars(KV_LDS - 1 - PS);
    }
    const u32x2 tag = *(const LAS u32x2*)(size_t)L_TAG, none = ~tag;
    if (ka) {
      bar();
      {
        f32x4 p[3][4];
        for (int v = 0; v < 4; ++v) {
          for (int hh = 0; hh < 2; ++hh) p[hh][v] = *(const LAS f32x4*)(size_t)(L_A + ((2 * hp + hh) * 4 + v) * 1024 + lane * 16);
          p[2][v] = *(const LAS f32x4*)(size_t)(L_Z + (hp * 8 + (lane & 4) + v) * 64 + (lane & 3) * 16);
        }
        sb();
        for (int k = 0; k < 3; ++k) p[k][0] = (p[k][0] + p[k][1]) + (p[k][2] + p[k][3]);
        st_sc1(pk8(p[0][0], p[1][0]), rws, lane * 16, WS_KV + ((j * 2 + hp) * 16 + rt) * 1024);
        st_sc1(__builtin_bit_cast(u32x4, p[2][0]), rws, lane < 8 ? lane * 16 : WS_END, WS_Z + ((j * 2 + hp) * 16 + rt) * 128);
      }
      static_assert(KV_LDS + 3 == PA, "");
      bar();
      vmwait<0>();
      set_flag(rws, lane * 16, F_KV + (j * 2 + hp) * 16 + rt, tag);
      bar();
      u32x2 early = none;
      if (TO == 3) {
        __builtin_amdgcn_s_sleep(SLK);
        early = ld_flag(rws, fo, F_KV + (j * 2 + hp) * 16);
        bar();
      }
      poll(rws, fo, F_KV + (j * 2 + hp) * 16, tag, early);
      u32x4 gv[16];
      f32x4 s[2], z[2];
      for (int hh = 0; hh < 2; ++hh) {
        z[hh] = __builtin_bit_cast(f32x4, ld_sc1(rws, (lane & 15) * 128 + (lane >> 4) * 16 + hh * 64, WS_Z + (j * 2 + hp) * 2048));
        s[hh] = f32x4{0, 0, 0, 0};
      }
      for (int v = 0; v < 16; ++v) gv[v] = ld_sc1(rws, lane * 16 + (v & 3) * 1024, WS_KV + ((j * 2 + hp) * 16 + (v & 12)) * 1024);
      const unsigned e = (lane >> 4) == ((lane & 15) >> 2) ? ((lane & 1) ? 0x3f800000u : 0x3f80u) : 0u, e0 = (lane & 2) ? 0u : e, e1 = (lane & 2) ? e : 0u;
      bar();
      sfor<0, 16>([&](auto vc) {
        constexpr int v = vc.v;
        if constexpr (v == RS) { asm volatile("" : "+v"(s[0]), "+v"(s[1]), "+v"(z[0]), "+v"(z[1])); bar(); }
        for (int hh = 0; hh < 2; ++hh) {
          s[hh] = mfma(__builtin_bit_cast(bf16x8, hh ? u32x4{0, 0, e0, e1} : u32x4{e0, e1, 0, 0}), __builtin_bit_cast(bf16x8, gv[v]), s[hh]);
          z[hh][v & 3] += __int_as_float(__builtin_amdgcn_update_dpp(0, __float_as_int(z[hh][v & 3]), 0x120 + (1 << (v >> 2)), 0xf, 0xf, false));
        }
      });
      for (int hh = 0; hh < 2; ++hh) *(LAS u32x4*)(size_t)(L_KVB + (2 * hp + hh) * 1024 + lane * 16) = pk8(s[hh], z[hh]);
      bar();
      if (hp == 0) return;
    }
    static_assert(EL + TO <= 4, "");
    bars(ka ? 4 - EL - TO : PB + 4 - EL - (KV_LDS - 1));
    u32x2 e = ld_flag(rws, fo, F_H + 512 + mt * 16);
    bars(EL);
    poll(rws, fo, F_H + 512 + mt * 16, tag, e);
    bars(4 - EL);
    e = ld_flag(rws, fo, F_H + 1024 + mt * 16);
    bars(EL);
    poll(rws, fo, F_H + 1024 + mt * 16, tag, e);
    bars(2 - EL);
    e = ld_flag(rws, fo, F_O + mt * 16);
    bars(EL);
    poll(rws, fo, F_O + mt * 16, tag, e);
    const unsigned gval = __builtin_amdgcn_raw_buffer_load_b32(rws, lane * 4, WS_G + mt * 256, 16);
    u32x2 all[2];
    if (b == 0) for (int k = 0; k < 2; ++k) all[k] = ld_flag(rws, (lane >> 3) * 2048 + (lane & 7) * 128, F_H + k * 256);
    bars(2);
    *(LAS unsigned*)(size_t)(L_G64 + lane * 4) = gval;
    bar();
    if (b == 0) {
      for (int k = 0; k < 2; ++k) poll(rws, (lane >> 3) * 2048 + (lane & 7) * 128, F_H + k * 256, tag, all[k]);
      __builtin_amdgcn_raw_buffer_store_b64(tag, rws, lane << 24, 0, 16);
    }
  }
}

extern "C" void kernel_launch(void* const* d_in, const int* in_sizes, int n_in,
                              void* d_out, int out_size, void* d_ws, size_t ws_size,
                              hipStream_t stream) {
  fused_kernel<<<256, 512, 0, stream>>>((const char*)d_in[0], (const char*)d_in[1], (const char*)d_in[2],
                                        (const char*)d_in[4], (const char*)d_in[5], (const char*)d_in[6],
                                        (const char*)d_in[7], (const char*)d_in[8],
                                        (__hip_bfloat16*)d_out, (char*)d_ws);
}
```
